```python
import torch

def _softmax(x, axis=-1):
    return torch.softmax(x, dim=axis)
def _gelu(x):
    return torch.nn.functional.gelu(x, approximate="tanh")
def _silu(x):
    return torch.nn.functional.silu(x)
def _sigmoid(x):
    return torch.sigmoid(x)
def _layernorm(x, axis=-1, eps=1e-5):
    m = x.mean(dim=axis, keepdim=True)
    v = x.var(dim=axis, keepdim=True, unbiased=False)
    return (x - m) / torch.sqrt(v + eps)
def _scatter_add(tgt, idx, src, axis=0):
    return tgt.clone().index_add_(axis, idx, src)
def _segment_sum(src, seg, n):
    out = torch.zeros((n,) + src.shape[1:], dtype=src.dtype)
    return out.index_add_(0, seg, src)
def _pad(x, pw):
    flat = tuple(v for pair in reversed(pw) for v in pair)
    return torch.nn.functional.pad(x, flat)

kernel_name = 'parallel_sum__sliding_block_cached__glu_gated_reglu__decode'

def setup_inputs():

    _w = (lambda a: (lambda t: t.to(torch.bfloat16) if t.is_floating_point() else t)(torch.as_tensor(a)))
    def _A_setup():
        B, Sq, D = 1, 1, 576
        Hq, Hkv, Sk, Dh = 32, 16, 32768, 128
        assert Hq % Hkv == 0, (Hq, Hkv)
        return {
            "x": _w((lambda *s: torch.randn(*s, dtype=torch.bfloat16) * 0.05)(B, Sq, D)),
            "k_cache": _w((lambda *s: torch.randn(*s, dtype=torch.bfloat16) * 1.6)(B, Hkv, Sk, Dh)),
            "v_cache": _w((lambda *s: torch.randn(*s, dtype=torch.bfloat16) * 0.8)(B, Hkv, Sk, Dh)),
            "wq": _w((lambda *s: torch.randn(*s, dtype=torch.bfloat16) * 1.6)(D, Hq * Dh) * (D ** -0.5)),
            "wo": _w((lambda *s: torch.randn(*s, dtype=torch.bfloat16) * 0.8)(Hq * Dh, D) * ((Hq * Dh) ** -0.5)),
        }
    _A_in = _A_setup()

    def _B_setup():
        B, S, D, F = 1, 1, 576, 4608
        return {
            "x": _w((lambda *s: torch.randn(*s, dtype=torch.bfloat16) * 0.05)(B, S, D)),
            "W_gate": _w((lambda *s: torch.randn(*s, dtype=torch.bfloat16) * 0.4)(D, F) * (2.0 / D) ** 0.5),
            "W_up": _w((lambda *s: torch.randn(*s, dtype=torch.bfloat16) * 0.4)(D, F) * (2.0 / D) ** 0.5),
            "W_down": _w((lambda *s: torch.randn(*s, dtype=torch.bfloat16) * 0.4)(F, D) * (1.0 / F) ** 0.5),
        }
    _B_in = _B_setup()

    return {
        "A_x": _A_in["x"],
        "A_k_cache": _A_in["k_cache"],
        "A_v_cache": _A_in["v_cache"],
        "A_wq": _A_in["wq"],
        "A_wo": _A_in["wo"],
        "B_x": _B_in["x"],
        "B_W_gate": _B_in["W_gate"],
        "B_W_up": _B_in["W_up"],
        "B_W_down": _B_in["W_down"],
    }

def reference(A_x, A_k_cache, A_v_cache, A_wq, A_wo, B_x, B_W_gate, B_W_up, B_W_down):
    def _A_ref(x, k_cache, v_cache, wq, wo):
        B, Sq, D = x.shape
        Hq, Dh = 32, 128
        Hkv, Sk = k_cache.shape[1], k_cache.shape[2]
        W = min(4096, max(256, Sk // 4))
        rep = Hq // Hkv
        q = (lambda x, *d: x.permute(*d))((x @ wq).reshape(B, Sq, Hq, Dh), 0, 2, 1, 3)
        k = (lambda x, n, axis: torch.repeat_interleave(x, n, dim=axis))(k_cache[:, :, Sk - W:, :], rep, axis=1)
        v = (lambda x, n, axis: torch.repeat_interleave(x, n, dim=axis))(v_cache[:, :, Sk - W:, :], rep, axis=1)
        scores = torch.einsum("bhqd,bhkd->bhqk", q, k) * (Dh ** -0.5)
        p = _softmax(scores, axis=-1)
        o = torch.einsum("bhqk,bhkd->bhqd", p, v)
        return (lambda x, *d: x.permute(*d))(o, 0, 2, 1, 3).reshape(B, Sq, Hq * Dh) @ wo

    def _B_ref(x, W_gate, W_up, W_down):
        gate = torch.einsum("bsd,df->bsf", x, W_gate)
        up = torch.einsum("bsd,df->bsf", x, W_up)
        gate = gate * (lambda x, dt: x.to(dt))(gate > 0, gate.dtype)
        h = gate * up
        return torch.einsum("bsf,fd->bsd", h, W_down)

    x = A_x
    xn = _layernorm(x, axis=-1)
    return x + _A_ref(xn, A_k_cache, A_v_cache, A_wq, A_wo) + _B_ref(xn, B_W_gate, B_W_up, B_W_down)
```

```cpp
#include <hip/hip_runtime.h>
#include <hip/hip_bf16.h>
#include <stdint.h>

typedef unsigned v4u __attribute__((ext_vector_type(4)));
typedef unsigned v3u __attribute__((ext_vector_type(3)));
typedef float f32x4 __attribute__((ext_vector_type(4)));
typedef __attribute__((ext_vector_type(2))) __bf16 bf16x2;

namespace {
constexpr int D = 576, HKV = 16, SK = 32768, DH = 128, WIN = 4096, FF = 4608;
constexpr int NWG = 256, NT = 512;
constexpr int NRED = 144;
constexpr int WS_Q = 4096;
constexpr int WS_A = WS_Q + 16 * 128 * 16;
constexpr int WS_F = WS_A + 256 * 16 * 9 * 16;
constexpr int WS_END = WS_F + NRED * 256 * 2 * 16;
constexpr int RSRC_W3 = 0x00020000;
constexpr int AUX_NT = 2, AUX_SC1 = 16;
constexpr unsigned SPIN_CAP = 1u << 20;

__device__ __forceinline__ float bf2f(unsigned short b) { return __builtin_bit_cast(float, (unsigned)b << 16); }
__device__ __forceinline__ float lo2f(unsigned w) { return __builtin_bit_cast(float, w << 16); }
__device__ __forceinline__ float hi2f(unsigned w) { return __builtin_bit_cast(float, w & 0xffff0000u); }
__device__ __forceinline__ unsigned short f2bf_bits(float f) {
  return __builtin_bit_cast(unsigned short, __float2bfloat16(f));
}
__device__ __forceinline__ float rbf(float f) { return bf2f(f2bf_bits(f)); }
__device__ __forceinline__ float dot2(unsigned k, unsigned q, float acc) {
  return __builtin_amdgcn_fdot2_f32_bf16(__builtin_bit_cast(bf16x2, k), __builtin_bit_cast(bf16x2, q), acc, false);
}
template <int CTRL> __device__ __forceinline__ float dpp(float x) {
  return __builtin_bit_cast(float, __builtin_amdgcn_mov_dpp(__builtin_bit_cast(int, x), CTRL, 0xf, 0xf, true));
}
constexpr int XOR1 = 0xB1, XOR2 = 0x4E, XOR7 = 0x141, ROR8 = 0x128, ROR4 = 0x124, ROR2 = 0x122, ROR1 = 0x121;
__device__ __forceinline__ float xrow16_max(float x) {
  auto s = __builtin_amdgcn_permlane16_swap(__float_as_uint(x), __float_as_uint(x), false, false);
  x = fmaxf(__uint_as_float(s[0]), __uint_as_float(s[1]));
  auto t = __builtin_amdgcn_permlane32_swap(__float_as_uint(x), __float_as_uint(x), false, false);
  return fmaxf(__uint_as_float(t[0]), __uint_as_float(t[1]));
}
__device__ __forceinline__ float xrow16_sum(float x) {
  auto s = __builtin_amdgcn_permlane16_swap(__float_as_uint(x), __float_as_uint(x), false, false);
  x = __uint_as_float(s[0]) + __uint_as_float(s[1]);
  auto t = __builtin_amdgcn_permlane32_swap(__float_as_uint(x), __float_as_uint(x), false, false);
  return __uint_as_float(t[0]) + __uint_as_float(t[1]);
}
__device__ __forceinline__ float row16_sum(float x) {
  x += dpp<ROR8>(x); x += dpp<ROR4>(x); x += dpp<ROR2>(x); x += dpp<ROR1>(x);
  return x;
}
__device__ __forceinline__ float wave_sum(float x) { return xrow16_sum(row16_sum(x)); }

__device__ __forceinline__ __amdgpu_buffer_rsrc_t mkrs(const void* p, unsigned bytes) {
  return __builtin_amdgcn_make_buffer_rsrc((void*)p, 0, (int)bytes, RSRC_W3);
}
__device__ __forceinline__ v4u ldnt128(__amdgpu_buffer_rsrc_t rs, int off) { return __builtin_amdgcn_raw_buffer_load_b128(rs, off, 0, AUX_NT); }
__device__ __forceinline__ v3u ldnt96(__amdgpu_buffer_rsrc_t rs, int off) { return __builtin_amdgcn_raw_buffer_load_b96(rs, off, 0, AUX_NT); }
__device__ __forceinline__ v4u ldsc(__amdgpu_buffer_rsrc_t rs, int off) { return __builtin_amdgcn_raw_buffer_load_b128(rs, off, 0, AUX_SC1); }
__device__ __forceinline__ void stgran(__amdgpu_buffer_rsrc_t rs, int off, float a, float b, unsigned t0, unsigned t1) {
  v4u g = {__float_as_uint(a), t0, __float_as_uint(b), t1};
  __builtin_amdgcn_raw_buffer_store_b128(g, rs, off, 0, AUX_SC1);
}
__device__ __forceinline__ v4u poll_gran(__amdgpu_buffer_rsrc_t rs, int off, bool active, unsigned t0, unsigned t1) {
  v4u g = ldsc(rs, off);
  for (unsigned spins = 0; spins < SPIN_CAP; ++spins) {
    const bool ok = !active || (g.y == t0 && g.w == t1);
    if (__all(ok)) break;
    __builtin_amdgcn_s_sleep(1);
    g = ldsc(rs, off);
  }
  return g;
}

struct Lds {
  float xn[D];
  float qred[8][16];
  unsigned qs[128];
  float pb[8][64];
  float mrg[8][2][132];
  float mred[8][24];
  float hs[18];
  float ga[16][18];
  float oc[16];
  float fr[6][D];
  float fin[8][4];
};

__global__ void __launch_bounds__(NT, 2)
fused_kernel(const unsigned short* __restrict__ x, const unsigned short* __restrict__ kc, const unsigned short* __restrict__ vc,
             const unsigned short* __restrict__ wq, const unsigned short* __restrict__ wo, const unsigned short* __restrict__ wg,
             const unsigned short* __restrict__ wu, const unsigned short* __restrict__ wd, unsigned short* __restrict__ out,
             unsigned char* ws) {
  __shared__ __attribute__((aligned(16))) Lds L;
  const int tid = threadIdx.x, lane = tid & 63, wave = __builtin_amdgcn_readfirstlane(tid >> 6);
  const int b = blockIdx.x, idx = b >> 3, g = (b & 7) * 2 + (idx >> 4), s = idx & 15, cb = (b & 7) * 32 + idx;
  const int r = lane >> 4, c = lane & 15;

  const __amdgpu_buffer_rsrc_t rs_ws = mkrs(ws, WS_END);
  const __amdgpu_buffer_rsrc_t rs_k = mkrs(kc, (unsigned)HKV * SK * DH * 2u);
  const __amdgpu_buffer_rsrc_t rs_v = mkrs(vc, (unsigned)HKV * SK * DH * 2u);
  const __amdgpu_buffer_rsrc_t rs_wq = mkrs(wq, D * 4096 * 2);
  const __amdgpu_buffer_rsrc_t rs_wo = mkrs(wo, 4096 * D * 2);
  const __amdgpu_buffer_rsrc_t rs_wgu = mkrs(wave < 4 ? wg : wu, D * FF * 2);
  const __amdgpu_buffer_rsrc_t rs_wd = mkrs(wd, FF * D * 2);

  const unsigned long long nonce = *(const volatile unsigned long long*)ws + 1ull;
  const unsigned t0 = (unsigned)nonce, t1 = (unsigned)(nonce >> 32);

  float xv[9];
  if (wave == 0) {
#pragma unroll
    for (int j = 0; j < 9; ++j) xv[j] = bf2f(x[lane + 64 * j]);
  }
  v4u wqr[3];
  {
    const int col = g * 256 + s * 16 + (lane & 1) * 8;
    const int row0 = wave * 72 + (lane >> 1);
    wqr[0] = ldnt128(rs_wq, (row0 * 4096 + col) * 2);
    wqr[1] = ldnt128(rs_wq, ((row0 + 32) * 4096 + col) * 2);
    wqr[2] = ldnt128(rs_wq, ((wave * 72 + 64 + ((lane >> 1) & 7)) * 4096 + col) * 2);
  }
  const int kvoff = (int)((((unsigned)g * SK + (SK - WIN) + s * 256 + wave * 32 + r) * DH + c * 8) * 2u);
  v4u kr[8];
#pragma unroll
  for (int i = 0; i < 8; ++i) kr[i] = ldnt128(rs_k, kvoff + i * 4 * DH * 2);
  __builtin_amdgcn_sched_barrier(0);

  if (wave == 0) {
    float sm = 0.f;
#pragma unroll
    for (int j = 0; j < 9; ++j) sm += xv[j];
    const float mean = wave_sum(sm) * (1.0f / D);
    float sv = 0.f;
#pragma unroll
    for (int j = 0; j < 9; ++j) { const float d = xv[j] - mean; sv += d * d; }
    const float var = wave_sum(sv) * (1.0f / D);
    const float mb = rbf(mean), vb = rbf(var);
    const float den = rbf(sqrtf(rbf(vb + 1e-5f)));
#pragma unroll
    for (int j = 0; j < 9; ++j) L.xn[lane + 64 * j] = rbf(rbf(xv[j] - mb) / den);
  }
  __syncthreads();

  {
    float acc[8];
#pragma unroll
    for (int k = 0; k < 8; ++k) acc[k] = 0.f;
    const int row0 = wave * 72 + (lane >> 1);
    const float c0 = L.xn[row0], c1 = L.xn[row0 + 32];
    const float c2 = lane < 16 ? L.xn[wave * 72 + 64 + ((lane >> 1) & 7)] : 0.f;
#pragma unroll
    for (int k = 0; k < 4; ++k) {
      acc[2 * k] = fmaf(c0, lo2f(wqr[0][k]), acc[2 * k]); acc[2 * k + 1] = fmaf(c0, hi2f(wqr[0][k]), acc[2 * k + 1]);
      acc[2 * k] = fmaf(c1, lo2f(wqr[1][k]), acc[2 * k]); acc[2 * k + 1] = fmaf(c1, hi2f(wqr[1][k]), acc[2 * k + 1]);
      acc[2 * k] = fmaf(c2, lo2f(wqr[2][k]), acc[2 * k]); acc[2 * k + 1] = fmaf(c2, hi2f(wqr[2][k]), acc[2 * k + 1]);
    }
#pragma unroll
    for (int k = 0; k < 8; ++k) {
      float a = acc[k];
      a += dpp<ROR8>(a); a += dpp<ROR4>(a); a += dpp<ROR2>(a);
      acc[k] = xrow16_sum(a);
    }
    if (lane < 2) {
#pragma unroll
      for (int k = 0; k < 8; ++k) L.qred[wave][lane * 8 + k] = acc[k];
    }
  }
  __syncthreads();
  if (tid < 8) {
    float a0 = 0.f, a1 = 0.f;
#pragma unroll
    for (int w = 0; w < 8; ++w) { a0 += L.qred[w][2 * tid]; a1 += L.qred[w][2 * tid + 1]; }
    stgran(rs_ws, WS_Q + (g * 128 + s * 8 + tid) * 16, rbf(a0), rbf(a1), t0, t1);
  }
  __builtin_amdgcn_sched_barrier(0);

  v4u vr[8];
#pragma unroll
  for (int i = 0; i < 8; ++i) vr[i] = ldnt128(rs_v, kvoff + i * 4 * DH * 2);
  __builtin_amdgcn_sched_barrier(0);

  if (wave < 2) {
    const v4u gq = poll_gran(rs_ws, WS_Q + (g * 128 + tid) * 16, true, t0, t1);
    L.qs[tid] = (gq.x >> 16) | (gq.z & 0xffff0000u);
  }
  __syncthreads();

  v3u gur[9];
  {
    const int part = lane & 3;
    const int row0 = (wave & 3) * 144 + (lane >> 2);
    const int off0 = part < 3 ? (row0 * FF + cb * 18 + part * 6) * 2 : 0x7ffffff0;
#pragma unroll
    for (int j = 0; j < 9; ++j) gur[j] = ldnt96(rs_wgu, part < 3 ? off0 + j * 16 * FF * 2 : off0);
  }
  __builtin_amdgcn_sched_barrier(0);

  {
    const v4u q0 = *(const v4u*)&L.qs[c * 4], q1 = *(const v4u*)&L.qs[64 + c * 4];
    float xs[16];
#pragma unroll
    for (int i = 0; i < 8; ++i) {
      float d0 = 0.f, d1 = 0.f;
#pragma unroll
      for (int k = 0; k < 4; ++k) { d0 = dot2(kr[i][k], q0[k], d0); d1 = dot2(kr[i][k], q1[k], d1); }
      xs[2 * i] = d0; xs[2 * i + 1] = d1;
    }
#define TR_STEP(HALF, CTRL, BIT) _Pragma("unroll") for (int h = 0; h < (HALF); ++h) { \
      const float lo_ = xs[h], hi_ = xs[h + (HALF)]; const float t1_ = lo_ + dpp<CTRL>(lo_), t2_ = hi_ + dpp<CTRL>(hi_); \
      xs[h] = (c & (BIT)) ? t2_ : t1_; }
    TR_STEP(8, ROR8, 8) TR_STEP(4, XOR7, 4) TR_STEP(2, XOR2, 2) TR_STEP(1, XOR1, 1)
#undef TR_STEP
    const float sc = rbf(rbf(xs[0]) * 0.08838834764831845f) * 1.4426950408889634f;
    float m = sc;
    m = fmaxf(m, dpp<XOR2>(m)); m = fmaxf(m, dpp<ROR4>(m)); m = fmaxf(m, dpp<ROR8>(m)); m = xrow16_max(m);
    const float p = __builtin_amdgcn_exp2f(sc - m);
    float l = p;
    l += dpp<XOR2>(l); l += dpp<ROR4>(l); l += dpp<ROR8>(l); l = xrow16_sum(l);
    L.pb[wave][lane] = p;
    __builtin_amdgcn_wave_barrier();
    asm volatile("s_waitcnt lgkmcnt(0)" ::: "memory");
    f32x4 pr[4];
#pragma unroll
    for (int u = 0; u < 4; ++u) pr[u] = *(const f32x4*)&L.pb[wave][r * 16 + u * 4];
    float a0[8], a1[8];
#pragma unroll
    for (int k = 0; k < 8; ++k) { a0[k] = 0.f; a1[k] = 0.f; }
#pragma unroll
    for (int i = 0; i < 8; ++i) {
      const float p0 = pr[i >> 1][(i & 1) * 2], p1 = pr[i >> 1][(i & 1) * 2 + 1];
#pragma unroll
      for (int k = 0; k < 4; ++k) {
        const float vl = lo2f(vr[i][k]), vh = hi2f(vr[i][k]);
        a0[2 * k] = fmaf(p0, vl, a0[2 * k]); a0[2 * k + 1] = fmaf(p0, vh, a0[2 * k + 1]);
        a1[2 * k] = fmaf(p1, vl, a1[2 * k]); a1[2 * k + 1] = fmaf(p1, vh, a1[2 * k + 1]);
      }
    }
#pragma unroll
    for (int k = 0; k < 8; ++k) { a0[k] = xrow16_sum(a0[k]); a1[k] = xrow16_sum(a1[k]); }
    if (r == 0) {
      *(f32x4*)&L.mrg[wave][0][4 + c * 8] = f32x4{a0[0], a0[1], a0[2], a0[3]};
      *(f32x4*)&L.mrg[wave][0][8 + c * 8] = f32x4{a0[4], a0[5], a0[6], a0[7]};
      *(f32x4*)&L.mrg[wave][1][4 + c * 8] = f32x4{a1[0], a1[1], a1[2], a1[3]};
      *(f32x4*)&L.mrg[wave][1][8 + c * 8] = f32x4{a1[4], a1[5], a1[6], a1[7]};
      if (c < 2) { L.mrg[wave][c][0] = m; L.mrg[wave][c][1] = l; }
    }
  }
  __syncthreads();
  if (tid < 144) {
    const int h = tid < 128 ? (tid >> 6) : ((tid - 128) >> 3);
    float M = -INFINITY;
#pragma unroll
    for (int w = 0; w < 8; ++w) M = fmaxf(M, L.mrg[w][h][0]);
    float e[8];
#pragma unroll
    for (int w = 0; w < 8; ++w) e[w] = __builtin_amdgcn_exp2f(L.mrg[w][h][0] - M);
    if (tid < 128) {
      const int dp = tid & 63;
      float o0 = 0.f, o1 = 0.f;
#pragma unroll
      for (int w = 0; w < 8; ++w) { o0 = fmaf(e[w], L.mrg[w][h][4 + 2 * dp], o0); o1 = fmaf(e[w], L.mrg[w][h][5 + 2 * dp], o1); }
      const int cons = h * 8 + (dp >> 3);
      stgran(rs_ws, WS_A + (((g * 16 + cons) * 16 + s) * 9 + 1 + (dp & 7)) * 16, o0, o1, t0, t1);
    } else {
      float Ls = 0.f;
#pragma unroll
      for (int w = 0; w < 8; ++w) Ls = fmaf(e[w], L.mrg[w][h][1], Ls);
      stgran(rs_ws, WS_A + (((g * 16 + (tid - 128)) * 16 + s) * 9) * 16, M, Ls, t0, t1);
    }
  }
  __builtin_amdgcn_sched_barrier(0);

  v4u wdr[3], wor[3];
  const int chunk = tid % 72, rg = tid / 72;
  {
    const bool act = tid < 432;
#pragma unroll
    for (int j = 0; j < 3; ++j) {
      const int row = rg + 6 * j;
      wdr[j] = ldnt128(rs_wd, act ? ((cb * 18 + row) * D + chunk * 8) * 2 : 0x7ffffff0);
      wor[j] = ldnt128(rs_wo, (act && row < 16) ? ((g * 256 + s * 16 + row) * D + chunk * 8) * 2 : 0x7ffffff0);
    }
  }
  __builtin_amdgcn_sched_barrier(0);

  {
    float acc[6];
#pragma unroll
    for (int k = 0; k < 6; ++k) acc[k] = 0.f;
    const int row0 = (wave & 3) * 144 + (lane >> 2);
#pragma unroll
    for (int j = 0; j < 9; ++j) {
      const float cf = L.xn[row0 + 16 * j];
#pragma unroll
      for (int k = 0; k < 3; ++k) {
        acc[2 * k] = fmaf(cf, lo2f(gur[j][k]), acc[2 * k]);
        acc[2 * k + 1] = fmaf(cf, hi2f(gur[j][k]), acc[2 * k + 1]);
      }
    }
#pragma unroll
    for (int k = 0; k < 6; ++k) {
      float a = acc[k];
      a += dpp<ROR4>(a); a += dpp<ROR8>(a);
      acc[k] = xrow16_sum(a);
    }
    if (lane < 3) {
#pragma unroll
      for (int k = 0; k < 6; ++k) L.mred[wave][lane * 6 + k] = acc[k];
    }
  }
  __syncthreads();
  if (tid < 18) {
    const float gt = rbf((L.mred[0][tid] + L.mred[1][tid]) + (L.mred[2][tid] + L.mred[3][tid]));
    const float up = rbf((L.mred[4][tid] + L.mred[5][tid]) + (L.mred[6][tid] + L.mred[7][tid]));
    L.hs[tid] = rbf((gt > 0.f ? gt : 0.f) * up);
  }
  if (wave >= 1 && wave < 4) {
    const int t = tid - 64;
    const v4u ga = poll_gran(rs_ws, WS_A + ((g * 16 + s) * 144 + t) * 16, t < 144, t0, t1);
    if (t < 144) { L.ga[t / 9][(t % 9) * 2] = __uint_as_float(ga.x); L.ga[t / 9][(t % 9) * 2 + 1] = __uint_as_float(ga.z); }
  }
  __syncthreads();
  if (tid < 16) {
    float M = -INFINITY;
#pragma unroll
    for (int p = 0; p < 16; ++p) M = fmaxf(M, L.ga[p][0]);
    float Ls = 0.f, o = 0.f;
#pragma unroll
    for (int p = 0; p < 16; ++p) { const float e = __builtin_amdgcn_exp2f(L.ga[p][0] - M); Ls = fmaf(e, L.ga[p][1], Ls); o = fmaf(e, L.ga[p][2 + tid], o); }
    L.oc[tid] = rbf(o / Ls);
  }
  __syncthreads();
  if (tid < 432) {
    float acc[8];
#pragma unroll
    for (int k = 0; k < 8; ++k) acc[k] = 0.f;
#pragma unroll
    for (int j = 0; j < 3; ++j) {
      const int row = rg + 6 * j;
      const float ch = L.hs[row], co = row < 16 ? L.oc[row] : 0.f;
#pragma unroll
      for (int k = 0; k < 4; ++k) {
        acc[2 * k] = fmaf(ch, lo2f(wdr[j][k]), acc[2 * k]); acc[2 * k + 1] = fmaf(ch, hi2f(wdr[j][k]), acc[2 * k + 1]);
        acc[2 * k] = fmaf(co, lo2f(wor[j][k]), acc[2 * k]); acc[2 * k + 1] = fmaf(co, hi2f(wor[j][k]), acc[2 * k + 1]);
      }
    }
    *(f32x4*)&L.fr[rg][chunk * 8] = f32x4{acc[0], acc[1], acc[2], acc[3]};
    *(f32x4*)&L.fr[rg][chunk * 8 + 4] = f32x4{acc[4], acc[5], acc[6], acc[7]};
  }
  __syncthreads();
  if (tid < 288) {
    float a0 = 0.f, a1 = 0.f;
#pragma unroll
    for (int q = 0; q < 6; ++q) { a0 += L.fr[q][2 * tid]; a1 += L.fr[q][2 * tid + 1]; }
    stgran(rs_ws, WS_F + (((tid >> 1) * 256 + b) * 2 + (tid & 1)) * 16, a0, a1, t0, t1);
  }
  if (b < NRED) {
    const v4u gf = poll_gran(rs_ws, WS_F + (b * 512 + tid) * 16, true, t0, t1);
    float a0 = __uint_as_float(gf.x), a1 = __uint_as_float(gf.z);
    a0 += dpp<XOR2>(a0); a0 += dpp<ROR4>(a0); a0 += dpp<ROR8>(a0); a0 = xrow16_sum(a0);
    a1 += dpp<XOR2>(a1); a1 += dpp<ROR4>(a1); a1 += dpp<ROR8>(a1); a1 = xrow16_sum(a1);
    if (lane < 2) { L.fin[wave][lane * 2] = a0; L.fin[wave][lane * 2 + 1] = a1; }
    __syncthreads();
    if (tid < 4) {
      float t = 0.f;
#pragma unroll
      for (int w = 0; w < 8; ++w) t += L.fin[w][tid];
      out[b * 4 + tid] = f2bf_bits(bf2f(x[b * 4 + tid]) + t);
    }
    if (b == 0 && tid == 0) *(volatile unsigned long long*)ws = nonce;
  }
}
}

extern "C" void kernel_launch(void* const* d_in, const int* in_sizes, int n_in, void* d_out, int out_size, void* d_ws,
                              size_t ws_size, hipStream_t stream) {
  (void)in_sizes; (void)n_in; (void)out_size; (void)ws_size;
  fused_kernel<<<NWG, NT, 0, stream>>>((const unsigned short*)d_in[0], (const unsigned short*)d_in[1], (const unsigned short*)d_in[2],
                                       (const unsigned short*)d_in[3], (const unsigned short*)d_in[4], (const unsigned short*)d_in[6],
                                       (const unsigned short*)d_in[7], (const unsigned short*)d_in[8], (unsigned short*)d_out,
                                       (unsigned char*)d_ws);
}
```

```cpp
#include <hip/hip_runtime.h>
#include <hip/hip_bf16.h>
#include <stdint.h>

typedef unsigned v4u __attribute__((ext_vector_type(4)));
typedef unsigned v2u __attribute__((ext_vector_type(2)));
typedef float f32x4 __attribute__((ext_vector_type(4)));
typedef __attribute__((ext_vector_type(2))) __bf16 bf16x2;

namespace {
constexpr int D = 576, HKV = 16, SK = 32768, DH = 128, WIN = 4096, FF = 4608;
constexpr int NWG = 256, NT = 512;
constexpr int NRED = 144;
constexpr int WS_Q = 4096;
constexpr int WS_G = WS_Q + 16 * 512 * 16;
constexpr int WS_A = WS_G + 256 * 144 * 16;
constexpr int WS_F = WS_A + 256 * 144 * 16;
constexpr int WS_END = WS_F + NRED * 512 * 16;
constexpr int NSTAMP = 12; constexpr int WS_STAMP = WS_END + 4096;
#define STAMP(i)
#define KI 2
constexpr int RSRC_W3 = 0x00020000;
constexpr int AUX_NT = 2, AUX_SC1 = 16;
constexpr unsigned SPIN_CAP = 1u << 20;
constexpr int OOB = 0x7ffffff0;

__device__ __forceinline__ float bf2f(unsigned short b) { return __builtin_bit_cast(float, (unsigned)b << 16); }
__device__ __forceinline__ float lo2f(unsigned w) { return __builtin_bit_cast(float, w << 16); }
__device__ __forceinline__ float hi2f(unsigned w) { return __builtin_bit_cast(float, w & 0xffff0000u); }
__device__ __forceinline__ unsigned short f2bf_bits(float f) { return __builtin_bit_cast(unsigned short, __float2bfloat16(f)); }
__device__ __forceinline__ float rbf(float f) { return bf2f(f2bf_bits(f)); }
__device__ __forceinline__ float dot2(unsigned k, unsigned q, float acc) {
  return __builtin_amdgcn_fdot2_f32_bf16(__builtin_bit_cast(bf16x2, k), __builtin_bit_cast(bf16x2, q), acc, false);
}
template <int CTRL> __device__ __forceinline__ float dpp(float x) {
  return __builtin_bit_cast(float, __builtin_amdgcn_mov_dpp(__builtin_bit_cast(int, x), CTRL, 0xf, 0xf, true));
}
constexpr int XOR1 = 0xB1, XOR2 = 0x4E, XOR7 = 0x141, ROR8 = 0x128, ROR4 = 0x124, ROR2 = 0x122, ROR1 = 0x121;
__device__ __forceinline__ float xrow16_max(float x) {
  auto s = __builtin_amdgcn_permlane16_swap(__float_as_uint(x), __float_as_uint(x), false, false);
  x = fmaxf(__uint_as_float(s[0]), __uint_as_float(s[1]));
  auto t = __builtin_amdgcn_permlane32_swap(__float_as_uint(x), __float_as_uint(x), false, false);
  return fmaxf(__uint_as_float(t[0]), __uint_as_float(t[1]));
}
__device__ __forceinline__ float xrow16_sum(float x) {
  auto s = __builtin_amdgcn_permlane16_swap(__float_as_uint(x), __float_as_uint(x), false, false);
  x = __uint_as_float(s[0]) + __uint_as_float(s[1]);
  auto t = __builtin_amdgcn_permlane32_swap(__float_as_uint(x), __float_as_uint(x), false, false);
  return __uint_as_float(t[0]) + __uint_as_float(t[1]);
}
__device__ __forceinline__ float row16_sum(float x) {
  x += dpp<ROR8>(x); x += dpp<ROR4>(x); x += dpp<ROR2>(x); x += dpp<ROR1>(x);
  return x;
}
__device__ __forceinline__ float wave_sum(float x) { return xrow16_sum(row16_sum(x)); }
__device__ __forceinline__ __amdgpu_buffer_rsrc_t mkrs(const void* p, unsigned bytes) {
  return __builtin_amdgcn_make_buffer_rsrc((void*)p, 0, (int)bytes, RSRC_W3);
}
__device__ __forceinline__ v4u ldnt128(__amdgpu_buffer_rsrc_t rs, int off) { return __builtin_amdgcn_raw_buffer_load_b128(rs, off, 0, AUX_NT); }
__device__ __forceinline__ v4u ldsc(__amdgpu_buffer_rsrc_t rs, int off) { return __builtin_amdgcn_raw_buffer_load_b128(rs, off, 0, AUX_SC1); }
__device__ __forceinline__ void stgran(__amdgpu_buffer_rsrc_t rs, int off, float a, float b, unsigned t0, unsigned t1) {
  v4u g = {__float_as_uint(a), t0, __float_as_uint(b), t1};
  __builtin_amdgcn_raw_buffer_store_b128(g, rs, off, 0, AUX_SC1);
}
__device__ __forceinline__ v4u poll_gran(__amdgpu_buffer_rsrc_t rs, int off, bool active, unsigned t0, unsigned t1) {
  v4u g = ldsc(rs, off);
  for (unsigned spins = 0; spins < SPIN_CAP; ++spins) {
    const bool ok = !active || (g.y == t0 && g.w == t1);
    if (__all(ok)) break;
    __builtin_amdgcn_s_sleep(1);
    g = ldsc(rs, off);
  }
  return g;
}
__device__ __forceinline__ void fma8(float (&acc)[8], float c, const v4u& w) {
#pragma unroll
  for (int k = 0; k < 4; ++k) { acc[2 * k] = fmaf(c, lo2f(w[k]), acc[2 * k]); acc[2 * k + 1] = fmaf(c, hi2f(w[k]), acc[2 * k + 1]); }
}

struct Lds {
  float xn[D];
  float qred[8][64];
  float gr[2][24][144];
  float qp[4][256];
  unsigned qs[128];
  float pb[8][64];
  float mrg[8][2][132];
  float gh[8][36];
  float hs[18];
  float ga[16][18];
  float oc[16];
  float fr[6][D];
  float fin[8][4];
};

__global__ void __launch_bounds__(NT, 2)
fused_kernel(const unsigned short* __restrict__ x, const unsigned short* __restrict__ kc, const unsigned short* __restrict__ vc,
             const unsigned short* __restrict__ wq, const unsigned short* __restrict__ wo, const unsigned short* __restrict__ wg,
             const unsigned short* __restrict__ wu, const unsigned short* __restrict__ wd, unsigned short* __restrict__ out,
             unsigned char* ws) {
  __shared__ __attribute__((aligned(16))) Lds L;
  const int tid = threadIdx.x, lane = tid & 63, wave = __builtin_amdgcn_readfirstlane(tid >> 6);
  const int b = blockIdx.x, cb = (b & 7) * 32 + (b >> 3), g = cb >> 4, s = cb & 15;
  const int r = lane >> 4, c = lane & 15;

  const __amdgpu_buffer_rsrc_t rs_ws = mkrs(ws, WS_END);
  const __amdgpu_buffer_rsrc_t rs_k = mkrs(kc, (unsigned)HKV * SK * DH * 2u);
  const __amdgpu_buffer_rsrc_t rs_v = mkrs(vc, (unsigned)HKV * SK * DH * 2u);
  const __amdgpu_buffer_rsrc_t rs_wq = mkrs(wq, D * 4096 * 2);
  const __amdgpu_buffer_rsrc_t rs_wo = mkrs(wo, 4096 * D * 2);
  const __amdgpu_buffer_rsrc_t rs_wg = mkrs(wg, D * FF * 2);
  const __amdgpu_buffer_rsrc_t rs_wu = mkrs(wu, D * FF * 2);
  const __amdgpu_buffer_rsrc_t rs_wd = mkrs(wd, FF * D * 2);
  STAMP(0);

  unsigned short xr[9];
#pragma unroll
  for (int j = 0; j < 9; ++j) xr[j] = x[lane + 64 * j];
  const int qrow = (s >> 2) * 144 + (tid >> 3);
  v4u wqr[3];
  {
    const int o = (qrow * 4096 + g * 256 + (s & 3) * 64 + (tid & 7) * 8) * 2;
    wqr[0] = ldnt128(rs_wq, o);
    wqr[1] = ldnt128(rs_wq, o + 64 * 4096 * 2);
    wqr[2] = ldnt128(rs_wq, tid < 128 ? o + 128 * 4096 * 2 : OOB);
  }
  const v2u nraw = __builtin_amdgcn_raw_buffer_load_b64(rs_ws, 0, 0, 0);
  const int gch = tid % 18, grg = tid / 18;
  const int grow = (cb & 7) * 72 + grg;
  v4u wgr[3], wur[3];
  {
    const int o = (grow * FF + (cb >> 3) * 144 + gch * 8) * 2;
#pragma unroll
    for (int j = 0; j < 3; ++j) {
      wgr[j] = ldnt128(rs_wg, tid < 432 ? o + j * 24 * FF * 2 : OOB);
      wur[j] = ldnt128(rs_wu, tid < 432 ? o + j * 24 * FF * 2 : OOB);
    }
  }
  const int kvoff = (int)((((unsigned)g * SK + (SK - WIN) + s * 256 + wave * 32 + r) * DH + c * 8) * 2u);
  v4u kr[8], vr[8];
#pragma unroll
  for (int i = 0; i < KI; ++i) kr[i] = ldnt128(rs_k, kvoff + i * 4 * DH * 2);
  __builtin_amdgcn_sched_barrier(0);

  {
    float xv[9];
#pragma unroll
    for (int j = 0; j < 9; ++j) xv[j] = bf2f(xr[j]);
    float sm = 0.f;
#pragma unroll
    for (int j = 0; j < 9; ++j) sm += xv[j];
    const float mean = wave_sum(sm) * (1.0f / D);
    float sv = 0.f;
#pragma unroll
    for (int j = 0; j < 9; ++j) { const float d = xv[j] - mean; sv += d * d; }
    const float var = wave_sum(sv) * (1.0f / D);
    const float mb = rbf(mean), vb = rbf(var);
    const float den = rbf(sqrtf(rbf(vb + 1e-5f)));
#pragma unroll
    for (int j = 0; j < 9; ++j) L.xn[lane + 64 * j] = rbf(rbf(xv[j] - mb) / den);
    __builtin_amdgcn_wave_barrier();
    asm volatile("s_waitcnt lgkmcnt(0)" ::: "memory");
  }
  const unsigned long long nonce = (((unsigned long long)nraw.y << 32) | nraw.x) + 1ull;
  const unsigned t0 = (unsigned)nonce, t1 = (unsigned)(nonce >> 32);
  STAMP(1);

  {
    float acc[8];
#pragma unroll
    for (int k = 0; k < 8; ++k) acc[k] = 0.f;
    fma8(acc, L.xn[qrow], wqr[0]);
    fma8(acc, L.xn[qrow + 64], wqr[1]);
    fma8(acc, tid < 128 ? L.xn[qrow + 128] : 0.f, wqr[2]);
#pragma unroll
    for (int k = 0; k < 8; ++k) { float a = acc[k]; a += dpp<ROR8>(a); acc[k] = xrow16_sum(a); }
    if (lane < 8) {
      *(f32x4*)&L.qred[wave][lane * 8] = f32x4{acc[0], acc[1], acc[2], acc[3]};
      *(f32x4*)&L.qred[wave][lane * 8 + 4] = f32x4{acc[4], acc[5], acc[6], acc[7]};
    }
  }
  if (tid < 432) {
    float ag[8], au[8];
#pragma unroll
    for (int k = 0; k < 8; ++k) { ag[k] = 0.f; au[k] = 0.f; }
#pragma unroll
    for (int j = 0; j < 3; ++j) { const float cf = L.xn[grow + 24 * j]; fma8(ag, cf, wgr[j]); fma8(au, cf, wur[j]); }
    *(f32x4*)&L.gr[0][grg][gch * 8] = f32x4{ag[0], ag[1], ag[2], ag[3]};
    *(f32x4*)&L.gr[0][grg][gch * 8 + 4] = f32x4{ag[4], ag[5], ag[6], ag[7]};
    *(f32x4*)&L.gr[1][grg][gch * 8] = f32x4{au[0], au[1], au[2], au[3]};
    *(f32x4*)&L.gr[1][grg][gch * 8 + 4] = f32x4{au[4], au[5], au[6], au[7]};
  }
  __syncthreads();
  if (tid < 32) {
    float a0 = 0.f, a1 = 0.f;
#pragma unroll
    for (int w = 0; w < 8; ++w) { a0 += L.qred[w][2 * tid]; a1 += L.qred[w][2 * tid + 1]; }
    stgran(rs_ws, WS_Q + (g * 512 + (s >> 2) * 128 + (s & 3) * 32 + tid) * 16, a0, a1, t0, t1);
  } else if (tid >= 64 && tid < 208) {
    const int col = tid - 64;
    float a0 = 0.f, a1 = 0.f;
#pragma unroll
    for (int q = 0; q < 24; ++q) { a0 += L.gr[0][q][col]; a1 += L.gr[1][q][col]; }
    stgran(rs_ws, WS_G + ((((cb & ~7) + col / 18) * 8 + (cb & 7)) * 18 + col % 18) * 16, a0, a1, t0, t1);
  }
  __builtin_amdgcn_sched_barrier(0);
  STAMP(2);

#pragma unroll
  for (int i = KI; i < 8; ++i) kr[i] = ldnt128(rs_k, kvoff + i * 4 * DH * 2);
#pragma unroll
  for (int i = 0; i < 8; ++i) vr[i] = ldnt128(rs_v, kvoff + i * 4 * DH * 2);
  __builtin_amdgcn_sched_barrier(0);

  {
    const v4u gq = poll_gran(rs_ws, WS_Q + (g * 512 + tid) * 16, true, t0, t1);
    *(v2u*)&L.qp[tid >> 7][(tid & 127) * 2] = v2u{gq.x, gq.z};
  }
  __syncthreads();
  if (tid < 128) {
    const float a0 = (L.qp[0][2 * tid] + L.qp[1][2 * tid]) + (L.qp[2][2 * tid] + L.qp[3][2 * tid]);
    const float a1 = (L.qp[0][2 * tid + 1] + L.qp[1][2 * tid + 1]) + (L.qp[2][2 * tid + 1] + L.qp[3][2 * tid + 1]);
    L.qs[tid] = (unsigned)f2bf_bits(a0) | ((unsigned)f2bf_bits(a1) << 16);
  }
  __syncthreads();
  STAMP(3);

  {
    const v4u q0 = *(const v4u*)&L.qs[c * 4], q1 = *(const v4u*)&L.qs[64 + c * 4];
    float xs[16];
#pragma unroll
    for (int i = 0; i < 8; ++i) {
      float d0 = 0.f, d1 = 0.f;
#pragma unroll
      for (int k = 0; k < 4; ++k) { d0 = dot2(kr[i][k], q0[k], d0); d1 = dot2(kr[i][k], q1[k], d1); }
      xs[2 * i] = d0; xs[2 * i + 1] = d1;
    }
#define TR_STEP(HALF, CTRL, BIT) _Pragma("unroll") for (int h = 0; h < (HALF); ++h) { \
      const float lo_ = xs[h], hi_ = xs[h + (HALF)]; const float t1_ = lo_ + dpp<CTRL>(lo_), t2_ = hi_ + dpp<CTRL>(hi_); \
      xs[h] = (c & (BIT)) ? t2_ : t1_; }
    TR_STEP(8, ROR8, 8) TR_STEP(4, XOR7, 4) TR_STEP(2, XOR2, 2) TR_STEP(1, XOR1, 1)
#undef TR_STEP
    const float sc = rbf(rbf(xs[0]) * 0.08838834764831845f) * 1.4426950408889634f;
    float m = sc;
    m = fmaxf(m, dpp<XOR2>(m)); m = fmaxf(m, dpp<ROR4>(m)); m = fmaxf(m, dpp<ROR8>(m)); m = xrow16_max(m);
    const float p = __builtin_amdgcn_exp2f(sc - m);
    float l = p;
    l += dpp<XOR2>(l); l += dpp<ROR4>(l); l += dpp<ROR8>(l); l = xrow16_sum(l);
    L.pb[wave][lane] = p;
    __builtin_amdgcn_wave_barrier();
    asm volatile("s_waitcnt lgkmcnt(0)" ::: "memory");
    f32x4 pr[4];
#pragma unroll
    for (int u = 0; u < 4; ++u) pr[u] = *(const f32x4*)&L.pb[wave][r * 16 + u * 4];
    float a0[8], a1[8];
#pragma unroll
    for (int k = 0; k < 8; ++k) { a0[k] = 0.f; a1[k] = 0.f; }
#pragma unroll
    for (int i = 0; i < 8; ++i) {
      const float p0 = pr[i >> 1][(i & 1) * 2], p1 = pr[i >> 1][(i & 1) * 2 + 1];
      fma8(a0, p0, vr[i]); fma8(a1, p1, vr[i]);
    }
#pragma unroll
    for (int k = 0; k < 8; ++k) { a0[k] = xrow16_sum(a0[k]); a1[k] = xrow16_sum(a1[k]); }
    if (r == 0) {
      *(f32x4*)&L.mrg[wave][0][4 + c * 8] = f32x4{a0[0], a0[1], a0[2], a0[3]};
      *(f32x4*)&L.mrg[wave][0][8 + c * 8] = f32x4{a0[4], a0[5], a0[6], a0[7]};
      *(f32x4*)&L.mrg[wave][1][4 + c * 8] = f32x4{a1[0], a1[1], a1[2], a1[3]};
      *(f32x4*)&L.mrg[wave][1][8 + c * 8] = f32x4{a1[4], a1[5], a1[6], a1[7]};
      if (c < 2) { L.mrg[wave][c][0] = m; L.mrg[wave][c][1] = l; }
    }
  }
  v4u wdr[3], wor[3];
  const int chunk = tid % 72, rg = tid / 72;
  {
    const bool act = tid < 432;
#pragma unroll
    for (int j = 0; j < 3; ++j) {
      const int row = rg + 6 * j;
      wor[j] = ldnt128(rs_wo, (act && row < 16) ? ((cb * 16 + row) * D + chunk * 8) * 2 : OOB);
      wdr[j] = ldnt128(rs_wd, act ? ((cb * 18 + row) * D + chunk * 8) * 2 : OOB);
    }
  }
  __builtin_amdgcn_sched_barrier(0);
  __syncthreads();
  STAMP(4);
  if (tid < 144) {
    const int h = tid < 128 ? (tid >> 6) : ((tid - 128) >> 3);
    float M = -INFINITY;
#pragma unroll
    for (int w = 0; w < 8; ++w) M = fmaxf(M, L.mrg[w][h][0]);
    float e[8];
#pragma unroll
    for (int w = 0; w < 8; ++w) e[w] = __builtin_amdgcn_exp2f(L.mrg[w][h][0] - M);
    if (tid < 128) {
      const int dp = tid & 63;
      float o0 = 0.f, o1 = 0.f;
#pragma unroll
      for (int w = 0; w < 8; ++w) { o0 = fmaf(e[w], L.mrg[w][h][4 + 2 * dp], o0); o1 = fmaf(e[w], L.mrg[w][h][5 + 2 * dp], o1); }
      const int cons = h * 8 + (dp >> 3);
      stgran(rs_ws, WS_A + (((g * 16 + cons) * 16 + s) * 9 + 1 + (dp & 7)) * 16, o0, o1, t0, t1);
    } else {
      float Ls = 0.f;
#pragma unroll
      for (int w = 0; w < 8; ++w) Ls = fmaf(e[w], L.mrg[w][h][1], Ls);
      stgran(rs_ws, WS_A + (((g * 16 + (tid - 128)) * 16 + s) * 9) * 16, M, Ls, t0, t1);
    }
  } else if (tid >= 192 && tid < 384) {
    const int t = tid - 192;
    const v4u gg = poll_gran(rs_ws, WS_G + (cb * 144 + t) * 16, t < 144, t0, t1);
    if (t < 144) { L.gh[t / 18][(t % 18) * 2] = __uint_as_float(gg.x); L.gh[t / 18][(t % 18) * 2 + 1] = __uint_as_float(gg.z); }
  }
  STAMP(5);
  __syncthreads();
  if (tid < 18) {
    float gt = 0.f, up = 0.f;
#pragma unroll
    for (int p = 0; p < 8; ++p) { gt += L.gh[p][2 * tid]; up += L.gh[p][2 * tid + 1]; }
    gt = rbf(gt); up = rbf(up);
    L.hs[tid] = rbf((gt > 0.f ? gt : 0.f) * up);
  }
  STAMP(6);
  if (wave >= 1 && wave < 4) {
    const int t = tid - 64;
    const v4u ga = poll_gran(rs_ws, WS_A + (cb * 144 + t) * 16, t < 144, t0, t1);
    if (t < 144) { L.ga[t / 9][(t % 9) * 2] = __uint_as_float(ga.x); L.ga[t / 9][(t % 9) * 2 + 1] = __uint_as_float(ga.z); }
  }
  __syncthreads();
  if (tid < 16) {
    float M = -INFINITY;
#pragma unroll
    for (int p = 0; p < 16; ++p) M = fmaxf(M, L.ga[p][0]);
    float Ls = 0.f, o = 0.f;
#pragma unroll
    for (int p = 0; p < 16; ++p) { const float e = __builtin_amdgcn_exp2f(L.ga[p][0] - M); Ls = fmaf(e, L.ga[p][1], Ls); o = fmaf(e, L.ga[p][2 + tid], o); }
    L.oc[tid] = rbf(o / Ls);
  }
  __syncthreads();
  STAMP(7);
  if (tid < 432) {
    float acc[8];
#pragma unroll
    for (int k = 0; k < 8; ++k) acc[k] = 0.f;
#pragma unroll
    for (int j = 0; j < 3; ++j) {
      const int row = rg + 6 * j;
      fma8(acc, L.hs[row], wdr[j]);
      fma8(acc, row < 16 ? L.oc[row] : 0.f, wor[j]);
    }
    *(f32x4*)&L.fr[rg][chunk * 8] = f32x4{acc[0], acc[1], acc[2], acc[3]};
    *(f32x4*)&L.fr[rg][chunk * 8 + 4] = f32x4{acc[4], acc[5], acc[6], acc[7]};
  }
  __syncthreads();
  if (tid < 288) {
    float a0 = 0.f, a1 = 0.f;
#pragma unroll
    for (int q = 0; q < 6; ++q) { a0 += L.fr[q][2 * tid]; a1 += L.fr[q][2 * tid + 1]; }
    stgran(rs_ws, WS_F + (((tid >> 1) * 256 + b) * 2 + (tid & 1)) * 16, a0, a1, t0, t1);
  }
  STAMP(8);
  if (b < NRED) {
    const v4u gf = poll_gran(rs_ws, WS_F + (b * 512 + tid) * 16, true, t0, t1);
    float a0 = __uint_as_float(gf.x), a1 = __uint_as_float(gf.z);
    a0 += dpp<XOR2>(a0); a0 += dpp<ROR4>(a0); a0 += dpp<ROR8>(a0); a0 = xrow16_sum(a0);
    a1 += dpp<XOR2>(a1); a1 += dpp<ROR4>(a1); a1 += dpp<ROR8>(a1); a1 = xrow16_sum(a1);
    if (lane < 2) { L.fin[wave][lane * 2] = a0; L.fin[wave][lane * 2 + 1] = a1; }
    __syncthreads();
    if (tid < 4) {
      float t = 0.f;
#pragma unroll
      for (int w = 0; w < 8; ++w) t += L.fin[w][tid];
      out[b * 4 + tid] = f2bf_bits(bf2f(x[b * 4 + tid]) + t);
    }
    if (b == 0 && tid == 0) *(volatile unsigned long long*)ws = nonce;
  }
  STAMP(9); STAMP(10); STAMP(11);
}
}

extern "C" void kernel_launch(void* const* d_in, const int* in_sizes, int n_in, void* d_out, int out_size, void* d_ws,
                              size_t ws_size, hipStream_t stream) {
  (void)in_sizes; (void)n_in; (void)out_size; (void)ws_size;
  fused_kernel<<<NWG, NT, 0, stream>>>((const unsigned short*)d_in[0], (const unsigned short*)d_in[1], (const unsigned short*)d_in[2],
                                       (const unsigned short*)d_in[3], (const unsigned short*)d_in[4], (const unsigned short*)d_in[6],
                                       (const unsigned short*)d_in[7], (const unsigned short*)d_in[8], (unsigned short*)d_out,
                                       (unsigned char*)d_ws);
}
```

```cpp
#define PF_CODE 176
#include <hip/hip_runtime.h>
#include <hip/hip_bf16.h>
#include <stdint.h>

typedef unsigned v4u __attribute__((ext_vector_type(4)));
typedef unsigned v2u __attribute__((ext_vector_type(2)));
typedef float f32x4 __attribute__((ext_vector_type(4)));
typedef __attribute__((ext_vector_type(2))) __bf16 bf16x2;

namespace {
constexpr int D = 576, HKV = 16, SK = 32768, DH = 128, WIN = 4096, FF = 4608;
constexpr int NWG = 256, NT = 512;
constexpr int NRED = 144;
constexpr int WS_Q = 4096;
constexpr int WS_G = WS_Q + 16 * 512 * 16;
constexpr int WS_A = WS_G + 256 * 144 * 16;
constexpr int WS_F = WS_A + 256 * 144 * 16;
constexpr int WS_END = WS_F + NRED * 512 * 16;
constexpr int NSTAMP = 12; constexpr int WS_STAMP = WS_END + 4096;
#define STAMP(i)
#define KI 2
constexpr int RSRC_W3 = 0x00020000;
constexpr int AUX_NT = 2, AUX_SC1 = 16;
constexpr unsigned SPIN_CAP = 1u << 20;
constexpr int OOB = 0x7ffffff0;

__device__ __forceinline__ float bf2f(unsigned short b) { return __builtin_bit_cast(float, (unsigned)b << 16); }
__device__ __forceinline__ float lo2f(unsigned w) { return __builtin_bit_cast(float, w << 16); }
__device__ __forceinline__ float hi2f(unsigned w) { return __builtin_bit_cast(float, w & 0xffff0000u); }
__device__ __forceinline__ unsigned short f2bf_bits(float f) { return __builtin_bit_cast(unsigned short, __float2bfloat16(f)); }
__device__ __forceinline__ float rbf(float f) { return bf2f(f2bf_bits(f)); }
__device__ __forceinline__ float dot2(unsigned k, unsigned q, float acc) {
  return __builtin_amdgcn_fdot2_f32_bf16(__builtin_bit_cast(bf16x2, k), __builtin_bit_cast(bf16x2, q), acc, false);
}
template <int CTRL> __device__ __forceinline__ float dpp(float x) {
  return __builtin_bit_cast(float, __builtin_amdgcn_mov_dpp(__builtin_bit_cast(int, x), CTRL, 0xf, 0xf, true));
}
constexpr int XOR1 = 0xB1, XOR2 = 0x4E, XOR7 = 0x141, ROR8 = 0x128, ROR4 = 0x124, ROR2 = 0x122, ROR1 = 0x121;
__device__ __forceinline__ float xrow16_max(float x) {
  auto s = __builtin_amdgcn_permlane16_swap(__float_as_uint(x), __float_as_uint(x), false, false);
  x = fmaxf(__uint_as_float(s[0]), __uint_as_float(s[1]));
  auto t = __builtin_amdgcn_permlane32_swap(__float_as_uint(x), __float_as_uint(x), false, false);
  return fmaxf(__uint_as_float(t[0]), __uint_as_float(t[1]));
}
__device__ __forceinline__ float xrow16_sum(float x) {
  auto s = __builtin_amdgcn_permlane16_swap(__float_as_uint(x), __float_as_uint(x), false, false);
  x = __uint_as_float(s[0]) + __uint_as_float(s[1]);
  auto t = __builtin_amdgcn_permlane32_swap(__float_as_uint(x), __float_as_uint(x), false, false);
  return __uint_as_float(t[0]) + __uint_as_float(t[1]);
}
__device__ __forceinline__ float row16_sum(float x) {
  x += dpp<ROR8>(x); x += dpp<ROR4>(x); x += dpp<ROR2>(x); x += dpp<ROR1>(x);
  return x;
}
__device__ __forceinline__ float wave_sum(float x) { return xrow16_sum(row16_sum(x)); }
__device__ __forceinline__ __amdgpu_buffer_rsrc_t mkrs(const void* p, unsigned bytes) {
  return __builtin_amdgcn_make_buffer_rsrc((void*)p, 0, (int)bytes, RSRC_W3);
}
__device__ __forceinline__ v4u ldnt128(__amdgpu_buffer_rsrc_t rs, int off) { return __builtin_amdgcn_raw_buffer_load_b128(rs, off, 0, AUX_NT); }
__device__ __forceinline__ v4u ldsc(__amdgpu_buffer_rsrc_t rs, int off) { return __builtin_amdgcn_raw_buffer_load_b128(rs, off, 0, AUX_SC1); }
__device__ __forceinline__ void stgran(__amdgpu_buffer_rsrc_t rs, int off, float a, float b, unsigned t0, unsigned t1) {
  v4u g = {__float_as_uint(a), t0, __float_as_uint(b), t1};
  __builtin_amdgcn_raw_buffer_store_b128(g, rs, off, 0, AUX_SC1);
}
__device__ __forceinline__ v4u poll_gran(__amdgpu_buffer_rsrc_t rs, int off, bool active, unsigned t0, unsigned t1) {
  v4u g = ldsc(rs, off);
  for (unsigned spins = 0; spins < SPIN_CAP; ++spins) {
    const bool ok = !active || (g.y == t0 && g.w == t1);
    if (__all(ok)) break;
    __builtin_amdgcn_s_sleep(1);
    g = ldsc(rs, off);
  }
  return g;
}
__device__ __forceinline__ void fma8(float (&acc)[8], float c, const v4u& w) {
#pragma unroll
  for (int k = 0; k < 4; ++k) { acc[2 * k] = fmaf(c, lo2f(w[k]), acc[2 * k]); acc[2 * k + 1] = fmaf(c, hi2f(w[k]), acc[2 * k + 1]); }
}

struct Lds {
  float xn[D];
  float qred[8][64];
  float gr[2][24][144];
  float qp[4][256];
  unsigned qs[128];
  float pb[8][64];
  float mrg[8][2][132];
  float gh[8][36];
  float hs[18];
  float ga[16][18];
  float oc[16];
  float fr[6][D];
  float fin[8][4];
};

__global__ void __launch_bounds__(NT, 2)
fused_kernel(const unsigned short* __restrict__ x, const unsigned short* __restrict__ kc, const unsigned short* __restrict__ vc,
             const unsigned short* __restrict__ wq, const unsigned short* __restrict__ wo, const unsigned short* __restrict__ wg,
             const unsigned short* __restrict__ wu, const unsigned short* __restrict__ wd, unsigned short* __restrict__ out,
             unsigned char* ws) {
  __shared__ __attribute__((aligned(16))) Lds L;
  const int tid = threadIdx.x, lane = tid & 63, wave = __builtin_amdgcn_readfirstlane(tid >> 6);
  const int b = blockIdx.x, cb = (b & 7) * 32 + (b >> 3), g = cb >> 4, s = cb & 15;
  const int r = lane >> 4, c = lane & 15;

  const __amdgpu_buffer_rsrc_t rs_ws = mkrs(ws, WS_END);
  const __amdgpu_buffer_rsrc_t rs_k = mkrs(kc, (unsigned)HKV * SK * DH * 2u);
  const __amdgpu_buffer_rsrc_t rs_v = mkrs(vc, (unsigned)HKV * SK * DH * 2u);
  const __amdgpu_buffer_rsrc_t rs_wq = mkrs(wq, D * 4096 * 2);
  const __amdgpu_buffer_rsrc_t rs_wo = mkrs(wo, 4096 * D * 2);
  const __amdgpu_buffer_rsrc_t rs_wg = mkrs(wg, D * FF * 2);
  const __amdgpu_buffer_rsrc_t rs_wu = mkrs(wu, D * FF * 2);
  const __amdgpu_buffer_rsrc_t rs_wd = mkrs(wd, FF * D * 2);
  STAMP(0);
  unsigned pfc[3];
  {
    unsigned long long pc;
    asm volatile("s_getpc_b64 %0" : "=s"(pc));
    const unsigned* cp = (const unsigned*)(pc & ~127ull);
#pragma unroll
    for (int j = 0; j < 3; ++j) { const int li = lane + 64 * j; pfc[j] = cp[(li < PF_CODE ? li : 0) * 16]; }
  }

  unsigned short xr[9];
#pragma unroll
  for (int j = 0; j < 9; ++j) xr[j] = x[lane + 64 * j];
  const int qrow = (s >> 2) * 144 + (tid >> 3);
  v4u wqr[3];
  {
    const int o = (qrow * 4096 + g * 256 + (s & 3) * 64 + (tid & 7) * 8) * 2;
    wqr[0] = ldnt128(rs_wq, o);
    wqr[1] = ldnt128(rs_wq, o + 64 * 4096 * 2);
    wqr[2] = ldnt128(rs_wq, tid < 128 ? o + 128 * 4096 * 2 : OOB);
  }
  const v2u nraw = __builtin_amdgcn_raw_buffer_load_b64(rs_ws, 0, 0, 0);
  const int gch = tid % 18, grg = tid / 18;
  const int grow = (cb & 7) * 72 + grg;
  v4u wgr[3], wur[3];
  {
    const int o = (grow * FF + (cb >> 3) * 144 + gch * 8) * 2;
#pragma unroll
    for (int j = 0; j < 3; ++j) {
      wgr[j] = ldnt128(rs_wg, tid < 432 ? o + j * 24 * FF * 2 : OOB);
      wur[j] = ldnt128(rs_wu, tid < 432 ? o + j * 24 * FF * 2 : OOB);
    }
  }
  const int kvoff = (int)((((unsigned)g * SK + (SK - WIN) + s * 256 + wave * 32 + r) * DH + c * 8) * 2u);
  v4u kr[8], vr[8];
#pragma unroll
  for (int i = 0; i < KI; ++i) kr[i] = ldnt128(rs_k, kvoff + i * 4 * DH * 2);
  __builtin_amdgcn_sched_barrier(0);

  {
    float xv[9];
#pragma unroll
    for (int j = 0; j < 9; ++j) xv[j] = bf2f(xr[j]);
    float sm = 0.f;
#pragma unroll
    for (int j = 0; j < 9; ++j) sm += xv[j];
    const float mean = wave_sum(sm) * (1.0f / D);
    float sv = 0.f;
#pragma unroll
    for (int j = 0; j < 9; ++j) { const float d = xv[j] - mean; sv += d * d; }
    const float var = wave_sum(sv) * (1.0f / D);
    const float mb = rbf(mean), vb = rbf(var);
    const float den = rbf(sqrtf(rbf(vb + 1e-5f)));
#pragma unroll
    for (int j = 0; j < 9; ++j) L.xn[lane + 64 * j] = rbf(rbf(xv[j] - mb) / den);
    __builtin_amdgcn_wave_barrier();
    asm volatile("s_waitcnt lgkmcnt(0)" ::: "memory");
  }
  const unsigned long long nonce = (((unsigned long long)nraw.y << 32) | nraw.x) + 1ull;
  const unsigned t0 = (unsigned)nonce, t1 = (unsigned)(nonce >> 32);
  STAMP(1);
#pragma unroll
  for (int j = 0; j < 3; ++j) asm volatile("" :: "v"(pfc[j]));

  {
    float acc[8];
#pragma unroll
    for (int k = 0; k < 8; ++k) acc[k] = 0.f;
    fma8(acc, L.xn[qrow], wqr[0]);
    fma8(acc, L.xn[qrow + 64], wqr[1]);
    fma8(acc, tid < 128 ? L.xn[qrow + 128] : 0.f, wqr[2]);
#pragma unroll
    for (int k = 0; k < 8; ++k) { float a = acc[k]; a += dpp<ROR8>(a); acc[k] = xrow16_sum(a); }
    if (lane < 8) {
      *(f32x4*)&L.qred[wave][lane * 8] = f32x4{acc[0], acc[1], acc[2], acc[3]};
      *(f32x4*)&L.qred[wave][lane * 8 + 4] = f32x4{acc[4], acc[5], acc[6], acc[7]};
    }
  }
  if (tid < 432) {
    float ag[8], au[8];
#pragma unroll
    for (int k = 0; k < 8; ++k) { ag[k] = 0.f; au[k] = 0.f; }
#pragma unroll
    for (int j = 0; j < 3; ++j) { const float cf = L.xn[grow + 24 * j]; fma8(ag, cf, wgr[j]); fma8(au, cf, wur[j]); }
    *(f32x4*)&L.gr[0][grg][gch * 8] = f32x4{ag[0], ag[1], ag[2], ag[3]};
    *(f32x4*)&L.gr[0][grg][gch * 8 + 4] = f32x4{ag[4], ag[5], ag[6], ag[7]};
    *(f32x4*)&L.gr[1][grg][gch * 8] = f32x4{au[0], au[1], au[2], au[3]};
    *(f32x4*)&L.gr[1][grg][gch * 8 + 4] = f32x4{au[4], au[5], au[6], au[7]};
  }
  __syncthreads();
  if (tid < 32) {
    float a0 = 0.f, a1 = 0.f;
#pragma unroll
    for (int w = 0; w < 8; ++w) { a0 += L.qred[w][2 * tid]; a1 += L.qred[w][2 * tid + 1]; }
    stgran(rs_ws, WS_Q + (g * 512 + (s >> 2) * 128 + (s & 3) * 32 + tid) * 16, a0, a1, t0, t1);
  } else if (tid >= 64 && tid < 208) {
    const int col = tid - 64;
    float a0 = 0.f, a1 = 0.f;
#pragma unroll
    for (int q = 0; q < 24; ++q) { a0 += L.gr[0][q][col]; a1 += L.gr[1][q][col]; }
    stgran(rs_ws, WS_G + ((((cb & ~7) + col / 18) * 8 + (cb & 7)) * 18 + col % 18) * 16, a0, a1, t0, t1);
  }
  __builtin_amdgcn_sched_barrier(0);
  STAMP(2);

#pragma unroll
  for (int i = KI; i < 8; ++i) kr[i] = ldnt128(rs_k, kvoff + i * 4 * DH * 2);
#pragma unroll
  for (int i = 0; i < 8; ++i) vr[i] = ldnt128(rs_v, kvoff + i * 4 * DH * 2);
  __builtin_amdgcn_sched_barrier(0);

  {
    const v4u gq = poll_gran(rs_ws, WS_Q + (g * 512 + tid) * 16, true, t0, t1);
    *(v2u*)&L.qp[tid >> 7][(tid & 127) * 2] = v2u{gq.x, gq.z};
  }
  __syncthreads();
  if (tid < 128) {
    const float a0 = (L.qp[0][2 * tid] + L.qp[1][2 * tid]) + (L.qp[2][2 * tid] + L.qp[3][2 * tid]);
    const float a1 = (L.qp[0][2 * tid + 1] + L.qp[1][2 * tid + 1]) + (L.qp[2][2 * tid + 1] + L.qp[3][2 * tid + 1]);
    L.qs[tid] = (unsigned)f2bf_bits(a0) | ((unsigned)f2bf_bits(a1) << 16);
  }
  __syncthreads();
  STAMP(3);

  {
    const v4u q0 = *(const v4u*)&L.qs[c * 4], q1 = *(const v4u*)&L.qs[64 + c * 4];
    float xs[16];
#pragma unroll
    for (int i = 0; i < 8; ++i) {
      float d0 = 0.f, d1 = 0.f;
#pragma unroll
      for (int k = 0; k < 4; ++k) { d0 = dot2(kr[i][k], q0[k], d0); d1 = dot2(kr[i][k], q1[k], d1); }
      xs[2 * i] = d0; xs[2 * i + 1] = d1;
    }
#define TR_STEP(HALF, CTRL, BIT) _Pragma("unroll") for (int h = 0; h < (HALF); ++h) { \
      const float lo_ = xs[h], hi_ = xs[h + (HALF)]; const float t1_ = lo_ + dpp<CTRL>(lo_), t2_ = hi_ + dpp<CTRL>(hi_); \
      xs[h] = (c & (BIT)) ? t2_ : t1_; }
    TR_STEP(8, ROR8, 8) TR_STEP(4, XOR7, 4) TR_STEP(2, XOR2, 2) TR_STEP(1, XOR1, 1)
#undef TR_STEP
    const float sc = rbf(rbf(xs[0]) * 0.08838834764831845f) * 1.4426950408889634f;
    float m = sc;
    m = fmaxf(m, dpp<XOR2>(m)); m = fmaxf(m, dpp<ROR4>(m)); m = fmaxf(m, dpp<ROR8>(m)); m = xrow16_max(m);
    const float p = __builtin_amdgcn_exp2f(sc - m);
    float l = p;
    l += dpp<XOR2>(l); l += dpp<ROR4>(l); l += dpp<ROR8>(l); l = xrow16_sum(l);
    L.pb[wave][lane] = p;
    __builtin_amdgcn_wave_barrier();
    asm volatile("s_waitcnt lgkmcnt(0)" ::: "memory");
    f32x4 pr[4];
#pragma unroll
    for (int u = 0; u < 4; ++u) pr[u] = *(const f32x4*)&L.pb[wave][r * 16 + u * 4];
    float a0[8], a1[8];
#pragma unroll
    for (int k = 0; k < 8; ++k) { a0[k] = 0.f; a1[k] = 0.f; }
#pragma unroll
    for (int i = 0; i < 8; ++i) {
      const float p0 = pr[i >> 1][(i & 1) * 2], p1 = pr[i >> 1][(i & 1) * 2 + 1];
      fma8(a0, p0, vr[i]); fma8(a1, p1, vr[i]);
    }
#pragma unroll
    for (int k = 0; k < 8; ++k) { a0[k] = xrow16_sum(a0[k]); a1[k] = xrow16_sum(a1[k]); }
    if (r == 0) {
      *(f32x4*)&L.mrg[wave][0][4 + c * 8] = f32x4{a0[0], a0[1], a0[2], a0[3]};
      *(f32x4*)&L.mrg[wave][0][8 + c * 8] = f32x4{a0[4], a0[5], a0[6], a0[7]};
      *(f32x4*)&L.mrg[wave][1][4 + c * 8] = f32x4{a1[0], a1[1], a1[2], a1[3]};
      *(f32x4*)&L.mrg[wave][1][8 + c * 8] = f32x4{a1[4], a1[5], a1[6], a1[7]};
      if (c < 2) { L.mrg[wave][c][0] = m; L.mrg[wave][c][1] = l; }
    }
  }
  v4u wdr[3], wor[3];
  const int chunk = tid % 72, rg = tid / 72;
  {
    const bool act = tid < 432;
#pragma unroll
    for (int j = 0; j < 3; ++j) {
      const int row = rg + 6 * j;
      wor[j] = ldnt128(rs_wo, (act && row < 16) ? ((cb * 16 + row) * D + chunk * 8) * 2 : OOB);
      wdr[j] = ldnt128(rs_wd, act ? ((cb * 18 + row) * D + chunk * 8) * 2 : OOB);
    }
  }
  __builtin_amdgcn_sched_barrier(0);
  __syncthreads();
  STAMP(4);
  if (tid < 144) {
    const int h = tid < 128 ? (tid >> 6) : ((tid - 128) >> 3);
    float M = -INFINITY;
#pragma unroll
    for (int w = 0; w < 8; ++w) M = fmaxf(M, L.mrg[w][h][0]);
    float e[8];
#pragma unroll
    for (int w = 0; w < 8; ++w) e[w] = __builtin_amdgcn_exp2f(L.mrg[w][h][0] - M);
    if (tid < 128) {
      const int dp = tid & 63;
      float o0 = 0.f, o1 = 0.f;
#pragma unroll
      for (int w = 0; w < 8; ++w) { o0 = fmaf(e[w], L.mrg[w][h][4 + 2 * dp], o0); o1 = fmaf(e[w], L.mrg[w][h][5 + 2 * dp], o1); }
      const int cons = h * 8 + (dp >> 3);
      stgran(rs_ws, WS_A + (((g * 16 + cons) * 16 + s) * 9 + 1 + (dp & 7)) * 16, o0, o1, t0, t1);
    } else {
      float Ls = 0.f;
#pragma unroll
      for (int w = 0; w < 8; ++w) Ls = fmaf(e[w], L.mrg[w][h][1], Ls);
      stgran(rs_ws, WS_A + (((g * 16 + (tid - 128)) * 16 + s) * 9) * 16, M, Ls, t0, t1);
    }
  } else if (tid >= 192 && tid < 384) {
    const int t = tid - 192;
    const v4u gg = poll_gran(rs_ws, WS_G + (cb * 144 + t) * 16, t < 144, t0, t1);
    if (t < 144) { L.gh[t / 18][(t % 18) * 2] = __uint_as_float(gg.x); L.gh[t / 18][(t % 18) * 2 + 1] = __uint_as_float(gg.z); }
  }
  STAMP(5);
  __syncthreads();
  if (tid < 18) {
    float gt = 0.f, up = 0.f;
#pragma unroll
    for (int p = 0; p < 8; ++p) { gt += L.gh[p][2 * tid]; up += L.gh[p][2 * tid + 1]; }
    gt = rbf(gt); up = rbf(up);
    L.hs[tid] = rbf((gt > 0.f ? gt : 0.f) * up);
  }
  STAMP(6);
  if (wave >= 1 && wave < 4) {
    const int t = tid - 64;
    const v4u ga = poll_gran(rs_ws, WS_A + (cb * 144 + t) * 16, t < 144, t0, t1);
    if (t < 144) { L.ga[t / 9][(t % 9) * 2] = __uint_as_float(ga.x); L.ga[t / 9][(t % 9) * 2 + 1] = __uint_as_float(ga.z); }
  }
  __syncthreads();
  if (tid < 16) {
    float M = -INFINITY;
#pragma unroll
    for (int p = 0; p < 16; ++p) M = fmaxf(M, L.ga[p][0]);
    float Ls = 0.f, o = 0.f;
#pragma unroll
    for (int p = 0; p < 16; ++p) { const float e = __builtin_amdgcn_exp2f(L.ga[p][0] - M); Ls = fmaf(e, L.ga[p][1], Ls); o = fmaf(e, L.ga[p][2 + tid], o); }
    L.oc[tid] = rbf(o / Ls);
  }
  __syncthreads();
  STAMP(7);
  if (tid < 432) {
    float acc[8];
#pragma unroll
    for (int k = 0; k < 8; ++k) acc[k] = 0.f;
#pragma unroll
    for (int j = 0; j < 3; ++j) {
      const int row = rg + 6 * j;
      fma8(acc, L.hs[row], wdr[j]);
      fma8(acc, row < 16 ? L.oc[row] : 0.f, wor[j]);
    }
    *(f32x4*)&L.fr[rg][chunk * 8] = f32x4{acc[0], acc[1], acc[2], acc[3]};
    *(f32x4*)&L.fr[rg][chunk * 8 + 4] = f32x4{acc[4], acc[5], acc[6], acc[7]};
  }
  __syncthreads();
  if (tid < 288) {
    float a0 = 0.f, a1 = 0.f;
#pragma unroll
    for (int q = 0; q < 6; ++q) { a0 += L.fr[q][2 * tid]; a1 += L.fr[q][2 * tid + 1]; }
    stgran(rs_ws, WS_F + (((tid >> 1) * 256 + b) * 2 + (tid & 1)) * 16, a0, a1, t0, t1);
  }
  STAMP(8);
  if (b < NRED) {
    const v4u gf = poll_gran(rs_ws, WS_F + (b * 512 + tid) * 16, true, t0, t1);
    float a0 = __uint_as_float(gf.x), a1 = __uint_as_float(gf.z);
    a0 += dpp<XOR2>(a0); a0 += dpp<ROR4>(a0); a0 += dpp<ROR8>(a0); a0 = xrow16_sum(a0);
    a1 += dpp<XOR2>(a1); a1 += dpp<ROR4>(a1); a1 += dpp<ROR8>(a1); a1 = xrow16_sum(a1);
    if (lane < 2) { L.fin[wave][lane * 2] = a0; L.fin[wave][lane * 2 + 1] = a1; }
    __syncthreads();
    if (tid < 4) {
      float t = 0.f;
#pragma unroll
      for (int w = 0; w < 8; ++w) t += L.fin[w][tid];
      out[b * 4 + tid] = f2bf_bits(bf2f(x[b * 4 + tid]) + t);
    }
    if (b == 0 && tid == 0) *(volatile unsigned long long*)ws = nonce;
  }
  STAMP(9); STAMP(10); STAMP(11);
}
}

extern "C" void kernel_launch(void* const* d_in, const int* in_sizes, int n_in, void* d_out, int out_size, void* d_ws,
                              size_t ws_size, hipStream_t stream) {
  (void)in_sizes; (void)n_in; (void)out_size; (void)ws_size;
  fused_kernel<<<NWG, NT, 0, stream>>>((const unsigned short*)d_in[0], (const unsigned short*)d_in[1], (const unsigned short*)d_in[2],
                                       (const unsigned short*)d_in[3], (const unsigned short*)d_in[4], (const unsigned short*)d_in[6],
                                       (const unsigned short*)d_in[7], (const unsigned short*)d_in[8], (unsigned short*)d_out,
                                       (unsigned char*)d_ws);
}
```

```cpp
#define STAMPS 1
#include <stdio.h>
#include <hip/hip_runtime.h>
#include <hip/hip_bf16.h>
#include <stdint.h>

typedef unsigned v4u __attribute__((ext_vector_type(4)));
typedef unsigned v2u __attribute__((ext_vector_type(2)));
typedef float f32x4 __attribute__((ext_vector_type(4)));
typedef __attribute__((ext_vector_type(2))) __bf16 bf16x2;

namespace {
constexpr int D = 576, HKV = 16, SK = 32768, DH = 128, WIN = 4096, FF = 4608;
constexpr int NWG = 256, NT = 512;
constexpr int NRED = 144;
constexpr int WS_Q = 4096;
constexpr int WS_G = WS_Q + 16 * 512 * 16;
constexpr int WS_A = WS_G + 256 * 144 * 16;
constexpr int WS_F = WS_A + 256 * 144 * 16;
constexpr int WS_END = WS_F + NRED * 512 * 16;
constexpr int NSTAMP = 12; constexpr int WS_STAMP = WS_END + 4096;
#define STAMP(i) do { if (tid == 0) ((unsigned long long*)(ws + WS_STAMP))[b * NSTAMP + (i)] = __builtin_amdgcn_s_memrealtime(); } while (0)
#define KI 2
constexpr int RSRC_W3 = 0x00020000;
constexpr int AUX_NT = 2, AUX_SC1 = 16;
constexpr unsigned SPIN_CAP = 1u << 20;
constexpr int OOB = 0x7ffffff0;

__device__ __forceinline__ float bf2f(unsigned short b) { return __builtin_bit_cast(float, (unsigned)b << 16); }
__device__ __forceinline__ float lo2f(unsigned w) { return __builtin_bit_cast(float, w << 16); }
__device__ __forceinline__ float hi2f(unsigned w) { return __builtin_bit_cast(float, w & 0xffff0000u); }
__device__ __forceinline__ unsigned short f2bf_bits(float f) { return __builtin_bit_cast(unsigned short, __float2bfloat16(f)); }
__device__ __forceinline__ float rbf(float f) { return bf2f(f2bf_bits(f)); }
__device__ __forceinline__ float dot2(unsigned k, unsigned q, float acc) {
  return __builtin_amdgcn_fdot2_f32_bf16(__builtin_bit_cast(bf16x2, k), __builtin_bit_cast(bf16x2, q), acc, false);
}
template <int CTRL> __device__ __forceinline__ float dpp(float x) {
  return __builtin_bit_cast(float, __builtin_amdgcn_mov_dpp(__builtin_bit_cast(int, x), CTRL, 0xf, 0xf, true));
}
constexpr int XOR1 = 0xB1, XOR2 = 0x4E, XOR7 = 0x141, ROR8 = 0x128, ROR4 = 0x124, ROR2 = 0x122, ROR1 = 0x121;
__device__ __forceinline__ float xrow16_max(float x) {
  auto s = __builtin_amdgcn_permlane16_swap(__float_as_uint(x), __float_as_uint(x), false, false);
  x = fmaxf(__uint_as_float(s[0]), __uint_as_float(s[1]));
  auto t = __builtin_amdgcn_permlane32_swap(__float_as_uint(x), __float_as_uint(x), false, false);
  return fmaxf(__uint_as_float(t[0]), __uint_as_float(t[1]));
}
__device__ __forceinline__ float xrow16_sum(float x) {
  auto s = __builtin_amdgcn_permlane16_swap(__float_as_uint(x), __float_as_uint(x), false, false);
  x = __uint_as_float(s[0]) + __uint_as_float(s[1]);
  auto t = __builtin_amdgcn_permlane32_swap(__float_as_uint(x), __float_as_uint(x), false, false);
  return __uint_as_float(t[0]) + __uint_as_float(t[1]);
}
__device__ __forceinline__ float row16_sum(float x) {
  x += dpp<ROR8>(x); x += dpp<ROR4>(x); x += dpp<ROR2>(x); x += dpp<ROR1>(x);
  return x;
}
__device__ __forceinline__ float wave_sum(float x) { return xrow16_sum(row16_sum(x)); }
__device__ __forceinline__ __amdgpu_buffer_rsrc_t mkrs(const void* p, unsigned bytes) {
  return __builtin_amdgcn_make_buffer_rsrc((void*)p, 0, (int)bytes, RSRC_W3);
}
__device__ __forceinline__ v4u ldnt128(__amdgpu_buffer_rsrc_t rs, int off) { return __builtin_amdgcn_raw_buffer_load_b128(rs, off, 0, AUX_NT); }
__device__ __forceinline__ v4u ldsc(__amdgpu_buffer_rsrc_t rs, int off) { return __builtin_amdgcn_raw_buffer_load_b128(rs, off, 0, AUX_SC1); }
__device__ __forceinline__ void stgran(__amdgpu_buffer_rsrc_t rs, int off, float a, float b, unsigned t0, unsigned t1) {
  v4u g = {__float_as_uint(a), t0, __float_as_uint(b), t1};
  __builtin_amdgcn_raw_buffer_store_b128(g, rs, off, 0, AUX_SC1);
}
__device__ __forceinline__ v4u poll_gran(__amdgpu_buffer_rsrc_t rs, int off, bool active, unsigned t0, unsigned t1) {
  v4u g = ldsc(rs, off);
  for (unsigned spins = 0; spins < SPIN_CAP; ++spins) {
    const bool ok = !active || (g.y == t0 && g.w == t1);
    if (__all(ok)) break;
    __builtin_amdgcn_s_sleep(1);
    g = ldsc(rs, off);
  }
  return g;
}
__device__ __forceinline__ void fma8(float (&acc)[8], float c, const v4u& w) {
#pragma unroll
  for (int k = 0; k < 4; ++k) { acc[2 * k] = fmaf(c, lo2f(w[k]), acc[2 * k]); acc[2 * k + 1] = fmaf(c, hi2f(w[k]), acc[2 * k + 1]); }
}

struct Lds {
  float xn[D];
  float qred[8][64];
  float gr[2][24][144];
  float qp[4][256];
  unsigned qs[128];
  float pb[8][64];
  float mrg[8][2][132];
  float gh[8][36];
  float hs[18];
  float ga[16][18];
  float oc[16];
  float fr[6][D];
  float fin[8][4];
};

__global__ void __launch_bounds__(NT, 2)
fused_kernel(const unsigned short* __restrict__ x, const unsigned short* __restrict__ kc, const unsigned short* __restrict__ vc,
             const unsigned short* __restrict__ wq, const unsigned short* __restrict__ wo, const unsigned short* __restrict__ wg,
             const unsigned short* __restrict__ wu, const unsigned short* __restrict__ wd, unsigned short* __restrict__ out,
             unsigned char* ws) {
  __shared__ __attribute__((aligned(16))) Lds L;
  const int tid = threadIdx.x, lane = tid & 63, wave = __builtin_amdgcn_readfirstlane(tid >> 6);
  const int b = blockIdx.x, cb = (b & 7) * 32 + (b >> 3), g = cb >> 4, s = cb & 15;
  const int r = lane >> 4, c = lane & 15;

  const __amdgpu_buffer_rsrc_t rs_ws = mkrs(ws, WS_END);
  const __amdgpu_buffer_rsrc_t rs_k = mkrs(kc, (unsigned)HKV * SK * DH * 2u);
  const __amdgpu_buffer_rsrc_t rs_v = mkrs(vc, (unsigned)HKV * SK * DH * 2u);
  const __amdgpu_buffer_rsrc_t rs_wq = mkrs(wq, D * 4096 * 2);
  const __amdgpu_buffer_rsrc_t rs_wo = mkrs(wo, 4096 * D * 2);
  const __amdgpu_buffer_rsrc_t rs_wg = mkrs(wg, D * FF * 2);
  const __amdgpu_buffer_rsrc_t rs_wu = mkrs(wu, D * FF * 2);
  const __amdgpu_buffer_rsrc_t rs_wd = mkrs(wd, FF * D * 2);
  STAMP(0);

  unsigned short xr[9];
#pragma unroll
  for (int j = 0; j < 9; ++j) xr[j] = x[lane + 64 * j];
  const int qrow = (s >> 2) * 144 + (tid >> 3);
  v4u wqr[3];
  {
    const int o = (qrow * 4096 + g * 256 + (s & 3) * 64 + (tid & 7) * 8) * 2;
    wqr[0] = ldnt128(rs_wq, o);
    wqr[1] = ldnt128(rs_wq, o + 64 * 4096 * 2);
    wqr[2] = ldnt128(rs_wq, tid < 128 ? o + 128 * 4096 * 2 : OOB);
  }
  const v2u nraw = __builtin_amdgcn_raw_buffer_load_b64(rs_ws, 0, 0, 0);
  const int gch = tid % 18, grg = tid / 18;
  const int grow = (cb & 7) * 72 + grg;
  v4u wgr[3], wur[3];
  {
    const int o = (grow * FF + (cb >> 3) * 144 + gch * 8) * 2;
#pragma unroll
    for (int j = 0; j < 3; ++j) {
      wgr[j] = ldnt128(rs_wg, tid < 432 ? o + j * 24 * FF * 2 : OOB);
      wur[j] = ldnt128(rs_wu, tid < 432 ? o + j * 24 * FF * 2 : OOB);
    }
  }
  const int kvoff = (int)((((unsigned)g * SK + (SK - WIN) + s * 256 + wave * 32 + r) * DH + c * 8) * 2u);
  v4u kr[8], vr[8];
#pragma unroll
  for (int i = 0; i < KI; ++i) kr[i] = ldnt128(rs_k, kvoff + i * 4 * DH * 2);
  __builtin_amdgcn_sched_barrier(0);

  {
    float xv[9];
#pragma unroll
    for (int j = 0; j < 9; ++j) xv[j] = bf2f(xr[j]);
    float sm = 0.f;
#pragma unroll
    for (int j = 0; j < 9; ++j) sm += xv[j];
    const float mean = wave_sum(sm) * (1.0f / D);
    float sv = 0.f;
#pragma unroll
    for (int j = 0; j < 9; ++j) { const float d = xv[j] - mean; sv += d * d; }
    const float var = wave_sum(sv) * (1.0f / D);
    const float mb = rbf(mean), vb = rbf(var);
    const float den = rbf(sqrtf(rbf(vb + 1e-5f)));
#pragma unroll
    for (int j = 0; j < 9; ++j) L.xn[lane + 64 * j] = rbf(rbf(xv[j] - mb) / den);
    __builtin_amdgcn_wave_barrier();
    asm volatile("s_waitcnt lgkmcnt(0)" ::: "memory");
  }
  const unsigned long long nonce = (((unsigned long long)nraw.y << 32) | nraw.x) + 1ull;
  const unsigned t0 = (unsigned)nonce, t1 = (unsigned)(nonce >> 32);
  STAMP(1);

  {
    float acc[8];
#pragma unroll
    for (int k = 0; k < 8; ++k) acc[k] = 0.f;
    fma8(acc, L.xn[qrow], wqr[0]);
    fma8(acc, L.xn[qrow + 64], wqr[1]);
    fma8(acc, tid < 128 ? L.xn[qrow + 128] : 0.f, wqr[2]);
#pragma unroll
    for (int k = 0; k < 8; ++k) { float a = acc[k]; a += dpp<ROR8>(a); acc[k] = xrow16_sum(a); }
    if (lane < 8) {
      *(f32x4*)&L.qred[wave][lane * 8] = f32x4{acc[0], acc[1], acc[2], acc[3]};
      *(f32x4*)&L.qred[wave][lane * 8 + 4] = f32x4{acc[4], acc[5], acc[6], acc[7]};
    }
  }
  if (tid < 432) {
    float ag[8], au[8];
#pragma unroll
    for (int k = 0; k < 8; ++k) { ag[k] = 0.f; au[k] = 0.f; }
#pragma unroll
    for (int j = 0; j < 3; ++j) { const float cf = L.xn[grow + 24 * j]; fma8(ag, cf, wgr[j]); fma8(au, cf, wur[j]); }
    *(f32x4*)&L.gr[0][grg][gch * 8] = f32x4{ag[0], ag[1], ag[2], ag[3]};
    *(f32x4*)&L.gr[0][grg][gch * 8 + 4] = f32x4{ag[4], ag[5], ag[6], ag[7]};
    *(f32x4*)&L.gr[1][grg][gch * 8] = f32x4{au[0], au[1], au[2], au[3]};
    *(f32x4*)&L.gr[1][grg][gch * 8 + 4] = f32x4{au[4], au[5], au[6], au[7]};
  }
  __syncthreads();
  if (tid < 32) {
    float a0 = 0.f, a1 = 0.f;
#pragma unroll
    for (int w = 0; w < 8; ++w) { a0 += L.qred[w][2 * tid]; a1 += L.qred[w][2 * tid + 1]; }
    stgran(rs_ws, WS_Q + (g * 512 + (s >> 2) * 128 + (s & 3) * 32 + tid) * 16, a0, a1, t0, t1);
  } else if (tid >= 64 && tid < 208) {
    const int col = tid - 64;
    float a0 = 0.f, a1 = 0.f;
#pragma unroll
    for (int q = 0; q < 24; ++q) { a0 += L.gr[0][q][col]; a1 += L.gr[1][q][col]; }
    stgran(rs_ws, WS_G + ((((cb & ~7) + col / 18) * 8 + (cb & 7)) * 18 + col % 18) * 16, a0, a1, t0, t1);
  }
  __builtin_amdgcn_sched_barrier(0);
  STAMP(2);

#pragma unroll
  for (int i = KI; i < 8; ++i) kr[i] = ldnt128(rs_k, kvoff + i * 4 * DH * 2);
#pragma unroll
  for (int i = 0; i < 8; ++i) vr[i] = ldnt128(rs_v, kvoff + i * 4 * DH * 2);
  __builtin_amdgcn_sched_barrier(0);

  {
    const v4u gq = poll_gran(rs_ws, WS_Q + (g * 512 + tid) * 16, true, t0, t1);
    *(v2u*)&L.qp[tid >> 7][(tid & 127) * 2] = v2u{gq.x, gq.z};
  }
  __syncthreads();
  if (tid < 128) {
    const float a0 = (L.qp[0][2 * tid] + L.qp[1][2 * tid]) + (L.qp[2][2 * tid] + L.qp[3][2 * tid]);
    const float a1 = (L.qp[0][2 * tid + 1] + L.qp[1][2 * tid + 1]) + (L.qp[2][2 * tid + 1] + L.qp[3][2 * tid + 1]);
    L.qs[tid] = (unsigned)f2bf_bits(a0) | ((unsigned)f2bf_bits(a1) << 16);
  }
  __syncthreads();
  STAMP(3);

  {
    const v4u q0 = *(const v4u*)&L.qs[c * 4], q1 = *(const v4u*)&L.qs[64 + c * 4];
    float xs[16];
#pragma unroll
    for (int i = 0; i < 8; ++i) {
      float d0 = 0.f, d1 = 0.f;
#pragma unroll
      for (int k = 0; k < 4; ++k) { d0 = dot2(kr[i][k], q0[k], d0); d1 = dot2(kr[i][k], q1[k], d1); }
      xs[2 * i] = d0; xs[2 * i + 1] = d1;
    }
#define TR_STEP(HALF, CTRL, BIT) _Pragma("unroll") for (int h = 0; h < (HALF); ++h) { \
      const float lo_ = xs[h], hi_ = xs[h + (HALF)]; const float t1_ = lo_ + dpp<CTRL>(lo_), t2_ = hi_ + dpp<CTRL>(hi_); \
      xs[h] = (c & (BIT)) ? t2_ : t1_; }
    TR_STEP(8, ROR8, 8) TR_STEP(4, XOR7, 4) TR_STEP(2, XOR2, 2) TR_STEP(1, XOR1, 1)
#undef TR_STEP
    const float sc = rbf(rbf(xs[0]) * 0.08838834764831845f) * 1.4426950408889634f;
    float m = sc;
    m = fmaxf(m, dpp<XOR2>(m)); m = fmaxf(m, dpp<ROR4>(m)); m = fmaxf(m, dpp<ROR8>(m)); m = xrow16_max(m);
    const float p = __builtin_amdgcn_exp2f(sc - m);
    float l = p;
    l += dpp<XOR2>(l); l += dpp<ROR4>(l); l += dpp<ROR8>(l); l = xrow16_sum(l);
    L.pb[wave][lane] = p;
    __builtin_amdgcn_wave_barrier();
    asm volatile("s_waitcnt lgkmcnt(0)" ::: "memory");
    f32x4 pr[4];
#pragma unroll
    for (int u = 0; u < 4; ++u) pr[u] = *(const f32x4*)&L.pb[wave][r * 16 + u * 4];
    float a0[8], a1[8];
#pragma unroll
    for (int k = 0; k < 8; ++k) { a0[k] = 0.f; a1[k] = 0.f; }
#pragma unroll
    for (int i = 0; i < 8; ++i) {
      const float p0 = pr[i >> 1][(i & 1) * 2], p1 = pr[i >> 1][(i & 1) * 2 + 1];
      fma8(a0, p0, vr[i]); fma8(a1, p1, vr[i]);
    }
#pragma unroll
    for (int k = 0; k < 8; ++k) { a0[k] = xrow16_sum(a0[k]); a1[k] = xrow16_sum(a1[k]); }
    if (r == 0) {
      *(f32x4*)&L.mrg[wave][0][4 + c * 8] = f32x4{a0[0], a0[1], a0[2], a0[3]};
      *(f32x4*)&L.mrg[wave][0][8 + c * 8] = f32x4{a0[4], a0[5], a0[6], a0[7]};
      *(f32x4*)&L.mrg[wave][1][4 + c * 8] = f32x4{a1[0], a1[1], a1[2], a1[3]};
      *(f32x4*)&L.mrg[wave][1][8 + c * 8] = f32x4{a1[4], a1[5], a1[6], a1[7]};
      if (c < 2) { L.mrg[wave][c][0] = m; L.mrg[wave][c][1] = l; }
    }
  }
  v4u wdr[3], wor[3];
  const int chunk = tid % 72, rg = tid / 72;
  {
    const bool act = tid < 432;
#pragma unroll
    for (int j = 0; j < 3; ++j) {
      const int row = rg + 6 * j;
      wor[j] = ldnt128(rs_wo, (act && row < 16) ? ((cb * 16 + row) * D + chunk * 8) * 2 : OOB);
      wdr[j] = ldnt128(rs_wd, act ? ((cb * 18 + row) * D + chunk * 8) * 2 : OOB);
    }
  }
  __builtin_amdgcn_sched_barrier(0);
  __syncthreads();
  STAMP(4);
  if (tid < 144) {
    const int h = tid < 128 ? (tid >> 6) : ((tid - 128) >> 3);
    float M = -INFINITY;
#pragma unroll
    for (int w = 0; w < 8; ++w) M = fmaxf(M, L.mrg[w][h][0]);
    float e[8];
#pragma unroll
    for (int w = 0; w < 8; ++w) e[w] = __builtin_amdgcn_exp2f(L.mrg[w][h][0] - M);
    if (tid < 128) {
      const int dp = tid & 63;
      float o0 = 0.f, o1 = 0.f;
#pragma unroll
      for (int w = 0; w < 8; ++w) { o0 = fmaf(e[w], L.mrg[w][h][4 + 2 * dp], o0); o1 = fmaf(e[w], L.mrg[w][h][5 + 2 * dp], o1); }
      const int cons = h * 8 + (dp >> 3);
      stgran(rs_ws, WS_A + (((g * 16 + cons) * 16 + s) * 9 + 1 + (dp & 7)) * 16, o0, o1, t0, t1);
    } else {
      float Ls = 0.f;
#pragma unroll
      for (int w = 0; w < 8; ++w) Ls = fmaf(e[w], L.mrg[w][h][1], Ls);
      stgran(rs_ws, WS_A + (((g * 16 + (tid - 128)) * 16 + s) * 9) * 16, M, Ls, t0, t1);
    }
  } else if (tid >= 192 && tid < 384) {
    const int t = tid - 192;
    const v4u gg = poll_gran(rs_ws, WS_G + (cb * 144 + t) * 16, t < 144, t0, t1);
    if (t < 144) { L.gh[t / 18][(t % 18) * 2] = __uint_as_float(gg.x); L.gh[t / 18][(t % 18) * 2 + 1] = __uint_as_float(gg.z); }
  }
  STAMP(5);
  __syncthreads();
  if (tid < 18) {
    float gt = 0.f, up = 0.f;
#pragma unroll
    for (int p = 0; p < 8; ++p) { gt += L.gh[p][2 * tid]; up += L.gh[p][2 * tid + 1]; }
    gt = rbf(gt); up = rbf(up);
    L.hs[tid] = rbf((gt > 0.f ? gt : 0.f) * up);
  }
  STAMP(6);
  if (wave >= 1 && wave < 4) {
    const int t = tid - 64;
    const v4u ga = poll_gran(rs_ws, WS_A + (cb * 144 + t) * 16, t < 144, t0, t1);
    if (t < 144) { L.ga[t / 9][(t % 9) * 2] = __uint_as_float(ga.x); L.ga[t / 9][(t % 9) * 2 + 1] = __uint_as_float(ga.z); }
  }
  __syncthreads();
  if (tid < 16) {
    float M = -INFINITY;
#pragma unroll
    for (int p = 0; p < 16; ++p) M = fmaxf(M, L.ga[p][0]);
    float Ls = 0.f, o = 0.f;
#pragma unroll
    for (int p = 0; p < 16; ++p) { const float e = __builtin_amdgcn_exp2f(L.ga[p][0] - M); Ls = fmaf(e, L.ga[p][1], Ls); o = fmaf(e, L.ga[p][2 + tid], o); }
    L.oc[tid] = rbf(o / Ls);
  }
  __syncthreads();
  STAMP(7);
  if (tid < 432) {
    float acc[8];
#pragma unroll
    for (int k = 0; k < 8; ++k) acc[k] = 0.f;
#pragma unroll
    for (int j = 0; j < 3; ++j) {
      const int row = rg + 6 * j;
      fma8(acc, L.hs[row], wdr[j]);
      fma8(acc, row < 16 ? L.oc[row] : 0.f, wor[j]);
    }
    *(f32x4*)&L.fr[rg][chunk * 8] = f32x4{acc[0], acc[1], acc[2], acc[3]};
    *(f32x4*)&L.fr[rg][chunk * 8 + 4] = f32x4{acc[4], acc[5], acc[6], acc[7]};
  }
  __syncthreads();
  if (tid < 288) {
    float a0 = 0.f, a1 = 0.f;
#pragma unroll
    for (int q = 0; q < 6; ++q) { a0 += L.fr[q][2 * tid]; a1 += L.fr[q][2 * tid + 1]; }
    stgran(rs_ws, WS_F + (((tid >> 1) * 256 + b) * 2 + (tid & 1)) * 16, a0, a1, t0, t1);
  }
  STAMP(8);
  if (b < NRED) {
    const v4u gf = poll_gran(rs_ws, WS_F + (b * 512 + tid) * 16, true, t0, t1);
    float a0 = __uint_as_float(gf.x), a1 = __uint_as_float(gf.z);
    a0 += dpp<XOR2>(a0); a0 += dpp<ROR4>(a0); a0 += dpp<ROR8>(a0); a0 = xrow16_sum(a0);
    a1 += dpp<XOR2>(a1); a1 += dpp<ROR4>(a1); a1 += dpp<ROR8>(a1); a1 = xrow16_sum(a1);
    if (lane < 2) { L.fin[wave][lane * 2] = a0; L.fin[wave][lane * 2 + 1] = a1; }
    __syncthreads();
    if (tid < 4) {
      float t = 0.f;
#pragma unroll
      for (int w = 0; w < 8; ++w) t += L.fin[w][tid];
      out[b * 4 + tid] = f2bf_bits(bf2f(x[b * 4 + tid]) + t);
    }
    if (b == 0 && tid == 0) *(volatile unsigned long long*)ws = nonce;
  }
  STAMP(9); STAMP(10); STAMP(11);
}
__global__ void report_kernel(unsigned char* ws) {
  const unsigned long long* st = (const unsigned long long*)(ws + WS_STAMP);
  unsigned long long t0 = ~0ull;
  for (int b = 0; b < 256; ++b) t0 = st[b * NSTAMP] < t0 ? st[b * NSTAMP] : t0;
  for (int i = 0; i < 10; ++i) {
    unsigned long long mn = ~0ull, mx = 0, sum = 0;
    for (int b = 0; b < 256; ++b) { unsigned long long v = st[b * NSTAMP + i] - t0; mn = v < mn ? v : mn; mx = v > mx ? v : mx; sum += v; }
    printf("stamp %d: min %d avg %d max %d (x10ns)\n", i, (int)mn, (int)(sum / 256), (int)mx);
  }
}
}

extern "C" void kernel_launch(void* const* d_in, const int* in_sizes, int n_in, void* d_out, int out_size, void* d_ws,
                              size_t ws_size, hipStream_t stream) {
  (void)in_sizes; (void)n_in; (void)out_size; (void)ws_size;
  fused_kernel<<<NWG, NT, 0, stream>>>((const unsigned short*)d_in[0], (const unsigned short*)d_in[1], (const unsigned short*)d_in[2],
                                       (const unsigned short*)d_in[3], (const unsigned short*)d_in[4], (const unsigned short*)d_in[6],
                                       (const unsigned short*)d_in[7], (const unsigned short*)d_in[8], (unsigned short*)d_out,
                                       (unsigned char*)d_ws);
  report_kernel<<<1, 1, 0, stream>>>((unsigned char*)d_ws);
}
```

```cpp
#define THRESH 2200
#include <hip/hip_runtime.h>
#include <hip/hip_bf16.h>
#include <stdint.h>

typedef unsigned v4u __attribute__((ext_vector_type(4)));
typedef unsigned v2u __attribute__((ext_vector_type(2)));
typedef float f32x4 __attribute__((ext_vector_type(4)));
typedef __attribute__((ext_vector_type(2))) __bf16 bf16x2;

namespace {
constexpr int D = 576, HKV = 16, SK = 32768, DH = 128, WIN = 4096, FF = 4608;
constexpr int NWG = 256, NT = 512;
constexpr int NRED = 144;
constexpr int WS_Q = 4096;
constexpr int WS_G = WS_Q + 16 * 512 * 16;
constexpr int WS_A = WS_G + 256 * 144 * 16;
constexpr int WS_F = WS_A + 256 * 144 * 16;
constexpr int WS_END = WS_F + NRED * 512 * 16;
constexpr int NSTAMP = 12; constexpr int WS_STAMP = WS_END + 4096;
#define STAMP(i)
#define KI 2
constexpr int RSRC_W3 = 0x00020000;
constexpr int AUX_NT = 2, AUX_SC1 = 16;
constexpr unsigned SPIN_CAP = 1u << 20;
constexpr int OOB = 0x7ffffff0;

__device__ __forceinline__ float bf2f(unsigned short b) { return __builtin_bit_cast(float, (unsigned)b << 16); }
__device__ __forceinline__ float lo2f(unsigned w) { return __builtin_bit_cast(float, w << 16); }
__device__ __forceinline__ float hi2f(unsigned w) { return __builtin_bit_cast(float, w & 0xffff0000u); }
__device__ __forceinline__ unsigned short f2bf_bits(float f) { return __builtin_bit_cast(unsigned short, __float2bfloat16(f)); }
__device__ __forceinline__ float rbf(float f) { return bf2f(f2bf_bits(f)); }
__device__ __forceinline__ float dot2(unsigned k, unsigned q, float acc) {
  return __builtin_amdgcn_fdot2_f32_bf16(__builtin_bit_cast(bf16x2, k), __builtin_bit_cast(bf16x2, q), acc, false);
}
template <int CTRL> __device__ __forceinline__ float dpp(float x) {
  return __builtin_bit_cast(float, __builtin_amdgcn_mov_dpp(__builtin_bit_cast(int, x), CTRL, 0xf, 0xf, true));
}
constexpr int XOR1 = 0xB1, XOR2 = 0x4E, XOR7 = 0x141, ROR8 = 0x128, ROR4 = 0x124, ROR2 = 0x122, ROR1 = 0x121;
__device__ __forceinline__ float xrow16_max(float x) {
  auto s = __builtin_amdgcn_permlane16_swap(__float_as_uint(x), __float_as_uint(x), false, false);
  x = fmaxf(__uint_as_float(s[0]), __uint_as_float(s[1]));
  auto t = __builtin_amdgcn_permlane32_swap(__float_as_uint(x), __float_as_uint(x), false, false);
  return fmaxf(__uint_as_float(t[0]), __uint_as_float(t[1]));
}
__device__ __forceinline__ float xrow16_sum(float x) {
  auto s = __builtin_amdgcn_permlane16_swap(__float_as_uint(x), __float_as_uint(x), false, false);
  x = __uint_as_float(s[0]) + __uint_as_float(s[1]);
  auto t = __builtin_amdgcn_permlane32_swap(__float_as_uint(x), __float_as_uint(x), false, false);
  return __uint_as_float(t[0]) + __uint_as_float(t[1]);
}
__device__ __forceinline__ float row16_sum(float x) {
  x += dpp<ROR8>(x); x += dpp<ROR4>(x); x += dpp<ROR2>(x); x += dpp<ROR1>(x);
  return x;
}
__device__ __forceinline__ float wave_sum(float x) { return xrow16_sum(row16_sum(x)); }
__device__ __forceinline__ __amdgpu_buffer_rsrc_t mkrs(const void* p, unsigned bytes) {
  return __builtin_amdgcn_make_buffer_rsrc((void*)p, 0, (int)bytes, RSRC_W3);
}
__device__ __forceinline__ v4u ldnt128(__amdgpu_buffer_rsrc_t rs, int off) { return __builtin_amdgcn_raw_buffer_load_b128(rs, off, 0, AUX_NT); }
__device__ __forceinline__ v4u ldsc(__amdgpu_buffer_rsrc_t rs, int off) { return __builtin_amdgcn_raw_buffer_load_b128(rs, off, 0, AUX_SC1); }
__device__ __forceinline__ void stgran(__amdgpu_buffer_rsrc_t rs, int off, float a, float b, unsigned t0, unsigned t1) {
  v4u g = {__float_as_uint(a), t0, __float_as_uint(b), t1};
  __builtin_amdgcn_raw_buffer_store_b128(g, rs, off, 0, AUX_SC1);
}
__device__ __forceinline__ v4u poll_gran(__amdgpu_buffer_rsrc_t rs, int off, bool active, unsigned t0, unsigned t1) {
  v4u g = ldsc(rs, off);
  for (unsigned spins = 0; spins < SPIN_CAP; ++spins) {
    const bool ok = !active || (g.y == t0 && g.w == t1);
    if (__all(ok)) break;
    __builtin_amdgcn_s_sleep(1);
    g = ldsc(rs, off);
  }
  return g;
}
__device__ __forceinline__ void fma8(float (&acc)[8], float c, const v4u& w) {
#pragma unroll
  for (int k = 0; k < 4; ++k) { acc[2 * k] = fmaf(c, lo2f(w[k]), acc[2 * k]); acc[2 * k + 1] = fmaf(c, hi2f(w[k]), acc[2 * k + 1]); }
}

struct Lds {
  float xn[D];
  float qred[8][64];
  float gr[2][24][144];
  float qp[4][256];
  unsigned qs[128];
  float pb[8][64];
  float mrg[8][2][132];
  float gh[8][36];
  float hs[18];
  float ga[16][18];
  float oc[16];
  float fr[6][D];
  float fin[8][4];
};

__global__ void __launch_bounds__(NT, 2)
fused_kernel(const unsigned short* __restrict__ x, const unsigned short* __restrict__ kc, const unsigned short* __restrict__ vc,
             const unsigned short* __restrict__ wq, const unsigned short* __restrict__ wo, const unsigned short* __restrict__ wg,
             const unsigned short* __restrict__ wu, const unsigned short* __restrict__ wd, unsigned short* __restrict__ out,
             unsigned char* ws) {
  __shared__ __attribute__((aligned(16))) Lds L;
  const int tid = threadIdx.x, lane = tid & 63, wave = __builtin_amdgcn_readfirstlane(tid >> 6);
  const int b = blockIdx.x, cb = (b & 7) * 32 + (b >> 3), g = cb >> 4, s = cb & 15;
  const int r = lane >> 4, c = lane & 15;

  const __amdgpu_buffer_rsrc_t rs_ws = mkrs(ws, WS_END);
  const __amdgpu_buffer_rsrc_t rs_k = mkrs(kc, (unsigned)HKV * SK * DH * 2u);
  const __amdgpu_buffer_rsrc_t rs_v = mkrs(vc, (unsigned)HKV * SK * DH * 2u);
  const __amdgpu_buffer_rsrc_t rs_wq = mkrs(wq, D * 4096 * 2);
  const __amdgpu_buffer_rsrc_t rs_wo = mkrs(wo, 4096 * D * 2);
  const __amdgpu_buffer_rsrc_t rs_wg = mkrs(wg, D * FF * 2);
  const __amdgpu_buffer_rsrc_t rs_wu = mkrs(wu, D * FF * 2);
  const __amdgpu_buffer_rsrc_t rs_wd = mkrs(wd, FF * D * 2);
  STAMP(0);
  const unsigned long long tstart = __builtin_amdgcn_s_memrealtime();

  unsigned short xr[9];
#pragma unroll
  for (int j = 0; j < 9; ++j) xr[j] = x[lane + 64 * j];
  const int qrow = (s >> 2) * 144 + (tid >> 3);
  v4u wqr[3];
  {
    const int o = (qrow * 4096 + g * 256 + (s & 3) * 64 + (tid & 7) * 8) * 2;
    wqr[0] = ldnt128(rs_wq, o);
    wqr[1] = ldnt128(rs_wq, o + 64 * 4096 * 2);
    wqr[2] = ldnt128(rs_wq, tid < 128 ? o + 128 * 4096 * 2 : OOB);
  }
  const v2u nraw = __builtin_amdgcn_raw_buffer_load_b64(rs_ws, 0, 0, 0);
  const int gch = tid % 18, grg = tid / 18;
  const int grow = (cb & 7) * 72 + grg;
  v4u wgr[3], wur[3];
  {
    const int o = (grow * FF + (cb >> 3) * 144 + gch * 8) * 2;
#pragma unroll
    for (int j = 0; j < 3; ++j) {
      wgr[j] = ldnt128(rs_wg, tid < 432 ? o + j * 24 * FF * 2 : OOB);
      wur[j] = ldnt128(rs_wu, tid < 432 ? o + j * 24 * FF * 2 : OOB);
    }
  }
  const int kvoff = (int)((((unsigned)g * SK + (SK - WIN) + s * 256 + wave * 32 + r) * DH + c * 8) * 2u);
  v4u kr[8], vr[8];
#pragma unroll
  for (int i = 0; i < KI; ++i) kr[i] = ldnt128(rs_k, kvoff + i * 4 * DH * 2);
  __builtin_amdgcn_sched_barrier(0);

  {
    float xv[9];
#pragma unroll
    for (int j = 0; j < 9; ++j) xv[j] = bf2f(xr[j]);
    float sm = 0.f;
#pragma unroll
    for (int j = 0; j < 9; ++j) sm += xv[j];
    const float mean = wave_sum(sm) * (1.0f / D);
    float sv = 0.f;
#pragma unroll
    for (int j = 0; j < 9; ++j) { const float d = xv[j] - mean; sv += d * d; }
    const float var = wave_sum(sv) * (1.0f / D);
    const float mb = rbf(mean), vb = rbf(var);
    const float den = rbf(sqrtf(rbf(vb + 1e-5f)));
#pragma unroll
    for (int j = 0; j < 9; ++j) L.xn[lane + 64 * j] = rbf(rbf(xv[j] - mb) / den);
    __builtin_amdgcn_wave_barrier();
    asm volatile("s_waitcnt lgkmcnt(0)" ::: "memory");
  }
  const unsigned long long nonce = (((unsigned long long)nraw.y << 32) | nraw.x) + 1ull;
  const unsigned t0 = (unsigned)nonce, t1 = (unsigned)(nonce >> 32);
  STAMP(1);

  {
    float acc[8];
#pragma unroll
    for (int k = 0; k < 8; ++k) acc[k] = 0.f;
    fma8(acc, L.xn[qrow], wqr[0]);
    fma8(acc, L.xn[qrow + 64], wqr[1]);
    fma8(acc, tid < 128 ? L.xn[qrow + 128] : 0.f, wqr[2]);
#pragma unroll
    for (int k = 0; k < 8; ++k) { float a = acc[k]; a += dpp<ROR8>(a); acc[k] = xrow16_sum(a); }
    if (lane < 8) {
      *(f32x4*)&L.qred[wave][lane * 8] = f32x4{acc[0], acc[1], acc[2], acc[3]};
      *(f32x4*)&L.qred[wave][lane * 8 + 4] = f32x4{acc[4], acc[5], acc[6], acc[7]};
    }
  }
  if (tid < 432) {
    float ag[8], au[8];
#pragma unroll
    for (int k = 0; k < 8; ++k) { ag[k] = 0.f; au[k] = 0.f; }
#pragma unroll
    for (int j = 0; j < 3; ++j) { const float cf = L.xn[grow + 24 * j]; fma8(ag, cf, wgr[j]); fma8(au, cf, wur[j]); }
    *(f32x4*)&L.gr[0][grg][gch * 8] = f32x4{ag[0], ag[1], ag[2], ag[3]};
    *(f32x4*)&L.gr[0][grg][gch * 8 + 4] = f32x4{ag[4], ag[5], ag[6], ag[7]};
    *(f32x4*)&L.gr[1][grg][gch * 8] = f32x4{au[0], au[1], au[2], au[3]};
    *(f32x4*)&L.gr[1][grg][gch * 8 + 4] = f32x4{au[4], au[5], au[6], au[7]};
  }
  __syncthreads();
  if (tid < 32) {
    float a0 = 0.f, a1 = 0.f;
#pragma unroll
    for (int w = 0; w < 8; ++w) { a0 += L.qred[w][2 * tid]; a1 += L.qred[w][2 * tid + 1]; }
    stgran(rs_ws, WS_Q + (g * 512 + (s >> 2) * 128 + (s & 3) * 32 + tid) * 16, a0, a1, t0, t1);
  } else if (tid >= 64 && tid < 208) {
    const int col = tid - 64;
    float a0 = 0.f, a1 = 0.f;
#pragma unroll
    for (int q = 0; q < 24; ++q) { a0 += L.gr[0][q][col]; a1 += L.gr[1][q][col]; }
    stgran(rs_ws, WS_G + ((((cb & ~7) + col / 18) * 8 + (cb & 7)) * 18 + col % 18) * 16, a0, a1, t0, t1);
  }
  __builtin_amdgcn_sched_barrier(0);
  STAMP(2);

#pragma unroll
  for (int i = KI; i < 8; ++i) kr[i] = ldnt128(rs_k, kvoff + i * 4 * DH * 2);
#pragma unroll
  for (int i = 0; i < 8; ++i) vr[i] = ldnt128(rs_v, kvoff + i * 4 * DH * 2);
  __builtin_amdgcn_sched_barrier(0);

  {
    const v4u gq = poll_gran(rs_ws, WS_Q + (g * 512 + tid) * 16, true, t0, t1);
    *(v2u*)&L.qp[tid >> 7][(tid & 127) * 2] = v2u{gq.x, gq.z};
  }
  __syncthreads();
  if (tid < 128) {
    const float a0 = (L.qp[0][2 * tid] + L.qp[1][2 * tid]) + (L.qp[2][2 * tid] + L.qp[3][2 * tid]);
    const float a1 = (L.qp[0][2 * tid + 1] + L.qp[1][2 * tid + 1]) + (L.qp[2][2 * tid + 1] + L.qp[3][2 * tid + 1]);
    L.qs[tid] = (unsigned)f2bf_bits(a0) | ((unsigned)f2bf_bits(a1) << 16);
  }
  __syncthreads();
  STAMP(3);

  {
    const v4u q0 = *(const v4u*)&L.qs[c * 4], q1 = *(const v4u*)&L.qs[64 + c * 4];
    float xs[16];
#pragma unroll
    for (int i = 0; i < 8; ++i) {
      float d0 = 0.f, d1 = 0.f;
#pragma unroll
      for (int k = 0; k < 4; ++k) { d0 = dot2(kr[i][k], q0[k], d0); d1 = dot2(kr[i][k], q1[k], d1); }
      xs[2 * i] = d0; xs[2 * i + 1] = d1;
    }
#define TR_STEP(HALF, CTRL, BIT) _Pragma("unroll") for (int h = 0; h < (HALF); ++h) { \
      const float lo_ = xs[h], hi_ = xs[h + (HALF)]; const float t1_ = lo_ + dpp<CTRL>(lo_), t2_ = hi_ + dpp<CTRL>(hi_); \
      xs[h] = (c & (BIT)) ? t2_ : t1_; }
    TR_STEP(8, ROR8, 8) TR_STEP(4, XOR7, 4) TR_STEP(2, XOR2, 2) TR_STEP(1, XOR1, 1)
#undef TR_STEP
    const float sc = rbf(rbf(xs[0]) * 0.08838834764831845f) * 1.4426950408889634f;
    float m = sc;
    m = fmaxf(m, dpp<XOR2>(m)); m = fmaxf(m, dpp<ROR4>(m)); m = fmaxf(m, dpp<ROR8>(m)); m = xrow16_max(m);
    const float p = __builtin_amdgcn_exp2f(sc - m);
    float l = p;
    l += dpp<XOR2>(l); l += dpp<ROR4>(l); l += dpp<ROR8>(l); l = xrow16_sum(l);
    L.pb[wave][lane] = p;
    __builtin_amdgcn_wave_barrier();
    asm volatile("s_waitcnt lgkmcnt(0)" ::: "memory");
    f32x4 pr[4];
#pragma unroll
    for (int u = 0; u < 4; ++u) pr[u] = *(const f32x4*)&L.pb[wave][r * 16 + u * 4];
    float a0[8], a1[8];
#pragma unroll
    for (int k = 0; k < 8; ++k) { a0[k] = 0.f; a1[k] = 0.f; }
#pragma unroll
    for (int i = 0; i < 8; ++i) {
      const float p0 = pr[i >> 1][(i & 1) * 2], p1 = pr[i >> 1][(i & 1) * 2 + 1];
      fma8(a0, p0, vr[i]); fma8(a1, p1, vr[i]);
    }
#pragma unroll
    for (int k = 0; k < 8; ++k) { a0[k] = xrow16_sum(a0[k]); a1[k] = xrow16_sum(a1[k]); }
    if (r == 0) {
      *(f32x4*)&L.mrg[wave][0][4 + c * 8] = f32x4{a0[0], a0[1], a0[2], a0[3]};
      *(f32x4*)&L.mrg[wave][0][8 + c * 8] = f32x4{a0[4], a0[5], a0[6], a0[7]};
      *(f32x4*)&L.mrg[wave][1][4 + c * 8] = f32x4{a1[0], a1[1], a1[2], a1[3]};
      *(f32x4*)&L.mrg[wave][1][8 + c * 8] = f32x4{a1[4], a1[5], a1[6], a1[7]};
      if (c < 2) { L.mrg[wave][c][0] = m; L.mrg[wave][c][1] = l; }
    }
  }
  v4u wdr[3], wor[3];
  const int chunk = tid % 72, rg = tid / 72;
  {
    const bool act = tid < 432;
#pragma unroll
    for (int j = 0; j < 3; ++j) {
      const int row = rg + 6 * j;
      wor[j] = ldnt128(rs_wo, (act && row < 16) ? ((cb * 16 + row) * D + chunk * 8) * 2 : OOB);
      wdr[j] = ldnt128(rs_wd, act ? ((cb * 18 + row) * D + chunk * 8) * 2 : OOB);
    }
  }
  __builtin_amdgcn_sched_barrier(0);
  __syncthreads();
  STAMP(4);
  if (tid < 144) {
    const int h = tid < 128 ? (tid >> 6) : ((tid - 128) >> 3);
    float M = -INFINITY;
#pragma unroll
    for (int w = 0; w < 8; ++w) M = fmaxf(M, L.mrg[w][h][0]);
    float e[8];
#pragma unroll
    for (int w = 0; w < 8; ++w) e[w] = __builtin_amdgcn_exp2f(L.mrg[w][h][0] - M);
    if (tid < 128) {
      const int dp = tid & 63;
      float o0 = 0.f, o1 = 0.f;
#pragma unroll
      for (int w = 0; w < 8; ++w) { o0 = fmaf(e[w], L.mrg[w][h][4 + 2 * dp], o0); o1 = fmaf(e[w], L.mrg[w][h][5 + 2 * dp], o1); }
      const int cons = h * 8 + (dp >> 3);
      stgran(rs_ws, WS_A + (((g * 16 + cons) * 16 + s) * 9 + 1 + (dp & 7)) * 16, o0, o1, t0, t1);
    } else {
      float Ls = 0.f;
#pragma unroll
      for (int w = 0; w < 8; ++w) Ls = fmaf(e[w], L.mrg[w][h][1], Ls);
      stgran(rs_ws, WS_A + (((g * 16 + (tid - 128)) * 16 + s) * 9) * 16, M, Ls, t0, t1);
    }
  } else if (tid >= 192 && tid < 384) {
    const int t = tid - 192;
    const v4u gg = poll_gran(rs_ws, WS_G + (cb * 144 + t) * 16, t < 144, t0, t1);
    if (t < 144) { L.gh[t / 18][(t % 18) * 2] = __uint_as_float(gg.x); L.gh[t / 18][(t % 18) * 2 + 1] = __uint_as_float(gg.z); }
  }
  STAMP(5);
  __syncthreads();
  if (tid < 18) {
    float gt = 0.f, up = 0.f;
#pragma unroll
    for (int p = 0; p < 8; ++p) { gt += L.gh[p][2 * tid]; up += L.gh[p][2 * tid + 1]; }
    gt = rbf(gt); up = rbf(up);
    L.hs[tid] = rbf((gt > 0.f ? gt : 0.f) * up);
  }
  STAMP(6);
  if (wave >= 1 && wave < 4) {
    const int t = tid - 64;
    const v4u ga = poll_gran(rs_ws, WS_A + (cb * 144 + t) * 16, t < 144, t0, t1);
    if (t < 144) { L.ga[t / 9][(t % 9) * 2] = __uint_as_float(ga.x); L.ga[t / 9][(t % 9) * 2 + 1] = __uint_as_float(ga.z); }
  }
  __syncthreads();
  if (tid < 16) {
    float M = -INFINITY;
#pragma unroll
    for (int p = 0; p < 16; ++p) M = fmaxf(M, L.ga[p][0]);
    float Ls = 0.f, o = 0.f;
#pragma unroll
    for (int p = 0; p < 16; ++p) { const float e = __builtin_amdgcn_exp2f(L.ga[p][0] - M); Ls = fmaf(e, L.ga[p][1], Ls); o = fmaf(e, L.ga[p][2 + tid], o); }
    L.oc[tid] = rbf(o / Ls);
  }
  __syncthreads();
  STAMP(7);
  if (tid < 432) {
    float acc[8];
#pragma unroll
    for (int k = 0; k < 8; ++k) acc[k] = 0.f;
#pragma unroll
    for (int j = 0; j < 3; ++j) {
      const int row = rg + 6 * j;
      fma8(acc, L.hs[row], wdr[j]);
      fma8(acc, row < 16 ? L.oc[row] : 0.f, wor[j]);
    }
    *(f32x4*)&L.fr[rg][chunk * 8] = f32x4{acc[0], acc[1], acc[2], acc[3]};
    *(f32x4*)&L.fr[rg][chunk * 8 + 4] = f32x4{acc[4], acc[5], acc[6], acc[7]};
  }
  __syncthreads();
  if (tid < 288) {
    float a0 = 0.f, a1 = 0.f;
#pragma unroll
    for (int q = 0; q < 6; ++q) { a0 += L.fr[q][2 * tid]; a1 += L.fr[q][2 * tid + 1]; }
    stgran(rs_ws, WS_F + (((tid >> 1) * 256 + b) * 2 + (tid & 1)) * 16, a0, a1, t0, t1);
  }
  STAMP(8);
  if (b < NRED) {
    const v4u gf = poll_gran(rs_ws, WS_F + (b * 512 + tid) * 16, true, t0, t1);
    float a0 = __uint_as_float(gf.x), a1 = __uint_as_float(gf.z);
    a0 += dpp<XOR2>(a0); a0 += dpp<ROR4>(a0); a0 += dpp<ROR8>(a0); a0 = xrow16_sum(a0);
    a1 += dpp<XOR2>(a1); a1 += dpp<ROR4>(a1); a1 += dpp<ROR8>(a1); a1 = xrow16_sum(a1);
    if (lane < 2) { L.fin[wave][lane * 2] = a0; L.fin[wave][lane * 2 + 1] = a1; }
    __syncthreads();
    if (tid < 4) {
      float t = 0.f;
#pragma unroll
      for (int w = 0; w < 8; ++w) t += L.fin[w][tid];
      const unsigned long long tend = __builtin_amdgcn_s_memrealtime();
      out[b * 4 + tid] = (tend - tstart > THRESH) ? (unsigned short)0x7fc0 : f2bf_bits(bf2f(x[b * 4 + tid]) + t);
    }
    if (b == 0 && tid == 0) *(volatile unsigned long long*)ws = nonce;
  }
  STAMP(9); STAMP(10); STAMP(11);
}
}

extern "C" void kernel_launch(void* const* d_in, const int* in_sizes, int n_in, void* d_out, int out_size, void* d_ws,
                              size_t ws_size, hipStream_t stream) {
  (void)in_sizes; (void)n_in; (void)out_size; (void)ws_size;
  fused_kernel<<<NWG, NT, 0, stream>>>((const unsigned short*)d_in[0], (const unsigned short*)d_in[1], (const unsigned short*)d_in[2],
                                       (const unsigned short*)d_in[3], (const unsigned short*)d_in[4], (const unsigned short*)d_in[6],
                                       (const unsigned short*)d_in[7], (const unsigned short*)d_in[8], (unsigned short*)d_out,
                                       (unsigned char*)d_ws);
}
```

```cpp
#include <hip/hip_runtime.h>
#include <hip/hip_bf16.h>
#include <stdint.h>

typedef unsigned v4u __attribute__((ext_vector_type(4)));
typedef unsigned v2u __attribute__((ext_vector_type(2)));
typedef float f32x4 __attribute__((ext_vector_type(4)));
typedef __attribute__((ext_vector_type(2))) __bf16 bf16x2;

namespace {
constexpr int D = 576, HKV = 16, SK = 32768, DH = 128, WIN = 4096, FF = 4608;
constexpr int NWG = 256, NT = 512;
constexpr int NRED = 144;
constexpr int WS_Q = 4096;
constexpr int WS_G = WS_Q + 16 * 512 * 16;
constexpr int WS_A = WS_G + 256 * 144 * 16;
constexpr int WS_F = WS_A + 256 * 144 * 16;
constexpr int WS_END = WS_F + NRED * 512 * 16;
constexpr int NSTAMP = 12; constexpr int WS_STAMP = WS_END + 4096;
#define STAMP(i)
#define FSLEEP 32
#define ASLEEP 0
#define RSLEEP 1
#define KI 8
constexpr int RSRC_W3 = 0x00020000;
constexpr int AUX_NT = 2, AUX_SC1 = 16;
constexpr unsigned SPIN_CAP = 1u << 20;
constexpr int OOB = 0x7ffffff0;

__device__ __forceinline__ float bf2f(unsigned short b) { return __builtin_bit_cast(float, (unsigned)b << 16); }
__device__ __forceinline__ float lo2f(unsigned w) { return __builtin_bit_cast(float, w << 16); }
__device__ __forceinline__ float hi2f(unsigned w) { return __builtin_bit_cast(float, w & 0xffff0000u); }
__device__ __forceinline__ unsigned short f2bf_bits(float f) { return __builtin_bit_cast(unsigned short, __float2bfloat16(f)); }
__device__ __forceinline__ float rbf(float f) { return bf2f(f2bf_bits(f)); }
__device__ __forceinline__ unsigned cvtpk(float lo, float hi) {
  return (unsigned)f2bf_bits(lo) | ((unsigned)f2bf_bits(hi) << 16);
}
__device__ __forceinline__ float dot2(unsigned k, unsigned q, float acc) {
  return __builtin_amdgcn_fdot2_f32_bf16(__builtin_bit_cast(bf16x2, k), __builtin_bit_cast(bf16x2, q), acc, false);
}
template <int CTRL> __device__ __forceinline__ float dpp(float x) {
  return __builtin_bit_cast(float, __builtin_amdgcn_mov_dpp(__builtin_bit_cast(int, x), CTRL, 0xf, 0xf, true));
}
constexpr int XOR1 = 0xB1, XOR2 = 0x4E, XOR7 = 0x141, ROR8 = 0x128, ROR4 = 0x124, ROR2 = 0x122, ROR1 = 0x121;
__device__ __forceinline__ float xrow16_max(float x) {
  auto s = __builtin_amdgcn_permlane16_swap(__float_as_uint(x), __float_as_uint(x), false, false);
  x = fmaxf(__uint_as_float(s[0]), __uint_as_float(s[1]));
  auto t = __builtin_amdgcn_permlane32_swap(__float_as_uint(x), __float_as_uint(x), false, false);
  return fmaxf(__uint_as_float(t[0]), __uint_as_float(t[1]));
}
__device__ __forceinline__ float xrow16_sum(float x) {
  auto s = __builtin_amdgcn_permlane16_swap(__float_as_uint(x), __float_as_uint(x), false, false);
  x = __uint_as_float(s[0]) + __uint_as_float(s[1]);
  auto t = __builtin_amdgcn_permlane32_swap(__float_as_uint(x), __float_as_uint(x), false, false);
  return __uint_as_float(t[0]) + __uint_as_float(t[1]);
}
__device__ __forceinline__ float row16_sum(float x) {
  x += dpp<ROR8>(x); x += dpp<ROR4>(x); x += dpp<ROR2>(x); x += dpp<ROR1>(x);
  return x;
}
__device__ __forceinline__ float wave_sum(float x) { return xrow16_sum(row16_sum(x)); }
__device__ __forceinline__ __amdgpu_buffer_rsrc_t mkrs(const void* p, unsigned bytes) {
  return __builtin_amdgcn_make_buffer_rsrc((void*)p, 0, (int)bytes, RSRC_W3);
}
__device__ __forceinline__ v4u ldnt128(__amdgpu_buffer_rsrc_t rs, int off) { return __builtin_amdgcn_raw_buffer_load_b128(rs, off, 0, AUX_NT); }
__device__ __forceinline__ v4u ldsc(__amdgpu_buffer_rsrc_t rs, int off) { return __builtin_amdgcn_raw_buffer_load_b128(rs, off, 0, AUX_SC1); }
__device__ __forceinline__ void stgran(__amdgpu_buffer_rsrc_t rs, int off, float a, float b, unsigned t0, unsigned t1) {
  v4u g = {__float_as_uint(a), t0, __float_as_uint(b), t1};
  __builtin_amdgcn_raw_buffer_store_b128(g, rs, off, 0, AUX_SC1);
}
__device__ __forceinline__ v4u poll_gran(__amdgpu_buffer_rsrc_t rs, int off, bool active, unsigned t0, unsigned t1) {
  v4u g = ldsc(rs, off);
  bool ok = !active || (g.y == t0 && g.w == t1);
  for (unsigned spins = 0; spins < SPIN_CAP; ++spins) {
    if (__all(ok)) break;
    __builtin_amdgcn_s_sleep(RSLEEP);
    if (!ok) { g = ldsc(rs, off); ok = (g.y == t0 && g.w == t1); }
  }
  return g;
}
__device__ __forceinline__ void fma8(float (&acc)[8], float c, const v4u& w) {
#pragma unroll
  for (int k = 0; k < 4; ++k) { acc[2 * k] = fmaf(c, lo2f(w[k]), acc[2 * k]); acc[2 * k + 1] = fmaf(c, hi2f(w[k]), acc[2 * k + 1]); }
}

struct Lds {
  float xn[D];
  float qred[8][64];
  float gr[2][24][144];
  float qp[4][256];
  unsigned qs[128];
  float pb[8][64];
  float mrg[8][2][132];
  float gh[8][36];
  float hs[18];
  float ga[16][18];
  float oc[16];
  float fr[6][D];
  float fin[8][4];
};

__global__ void __launch_bounds__(NT, 2)
fused_kernel(const unsigned short* __restrict__ x, const unsigned short* __restrict__ kc, const unsigned short* __restrict__ vc,
             const unsigned short* __restrict__ wq, const unsigned short* __restrict__ wo, const unsigned short* __restrict__ wg,
             const unsigned short* __restrict__ wu, const unsigned short* __restrict__ wd, unsigned short* __restrict__ out,
             unsigned char* ws) {
  __shared__ __attribute__((aligned(16))) Lds L;
  const int tid = threadIdx.x, lane = tid & 63, wave = __builtin_amdgcn_readfirstlane(tid >> 6);
  const int b = blockIdx.x, cb = (b & 7) * 32 + (b >> 3), g = cb >> 4, s = cb & 15;
  const int r = lane >> 4, c = lane & 15;

  const __amdgpu_buffer_rsrc_t rs_ws = mkrs(ws, WS_END);
  const __amdgpu_buffer_rsrc_t rs_k = mkrs(kc, (unsigned)HKV * SK * DH * 2u);
  const __amdgpu_buffer_rsrc_t rs_v = mkrs(vc, (unsigned)HKV * SK * DH * 2u);
  const __amdgpu_buffer_rsrc_t rs_wq = mkrs(wq, D * 4096 * 2);
  const __amdgpu_buffer_rsrc_t rs_wo = mkrs(wo, 4096 * D * 2);
  const __amdgpu_buffer_rsrc_t rs_wg = mkrs(wg, D * FF * 2);
  const __amdgpu_buffer_rsrc_t rs_wu = mkrs(wu, D * FF * 2);
  const __amdgpu_buffer_rsrc_t rs_wd = mkrs(wd, FF * D * 2);
  STAMP(0);

  unsigned short xr[9];
#pragma unroll
  for (int j = 0; j < 9; ++j) xr[j] = x[lane + 64 * j];
  const int qrow = (s >> 2) * 144 + (tid >> 3);
  v4u wqr[3];
  {
    const int o = (qrow * 4096 + g * 256 + (s & 3) * 64 + (tid & 7) * 8) * 2;
    wqr[0] = ldnt128(rs_wq, o);
    wqr[1] = ldnt128(rs_wq, o + 64 * 4096 * 2);
    wqr[2] = ldnt128(rs_wq, tid < 128 ? o + 128 * 4096 * 2 : OOB);
  }
  const v2u nraw = __builtin_amdgcn_raw_buffer_load_b64(rs_ws, 0, 0, 0);
  const int gch = tid % 18, grg = tid / 18;
  const int grow = (cb & 7) * 72 + grg;
  v4u wgr[3], wur[3];
  {
    const int o = (grow * FF + (cb >> 3) * 144 + gch * 8) * 2;
#pragma unroll
    for (int j = 0; j < 3; ++j) {
      wgr[j] = ldnt128(rs_wg, tid < 432 ? o + j * 24 * FF * 2 : OOB);
      wur[j] = ldnt128(rs_wu, tid < 432 ? o + j * 24 * FF * 2 : OOB);
    }
  }
  const int kvoff = (int)((((unsigned)g * SK + (SK - WIN) + s * 256 + wave * 32 + r) * DH + c * 8) * 2u);
  v4u kr[8], vr[8];
#pragma unroll
  for (int i = 0; i < KI; ++i) kr[i] = ldnt128(rs_k, kvoff + i * 4 * DH * 2);
  __builtin_amdgcn_sched_barrier(0);

  {
    float xv[9];
#pragma unroll
    for (int j = 0; j < 9; ++j) xv[j] = bf2f(xr[j]);
    float sm = 0.f;
#pragma unroll
    for (int j = 0; j < 9; ++j) sm += xv[j];
    const float mean = wave_sum(sm) * (1.0f / D);
    float sv = 0.f;
#pragma unroll
    for (int j = 0; j < 9; ++j) { const float d = xv[j] - mean; sv += d * d; }
    const float var = wave_sum(sv) * (1.0f / D);
    const float mb = rbf(mean), vb = rbf(var);
    const float den = rbf(sqrtf(rbf(vb + 1e-5f)));
#pragma unroll
    for (int j = 0; j < 9; ++j) L.xn[lane + 64 * j] = rbf(rbf(xv[j] - mb) / den);
    __builtin_amdgcn_wave_barrier();
    asm volatile("s_waitcnt lgkmcnt(0)" ::: "memory");
  }
  const unsigned long long nonce = (((unsigned long long)nraw.y << 32) | nraw.x) + 1ull;
  const unsigned t0 = (unsigned)nonce, t1 = (unsigned)(nonce >> 32);
  STAMP(1);

  {
    float acc[8];
#pragma unroll
    for (int k = 0; k < 8; ++k) acc[k] = 0.f;
    fma8(acc, L.xn[qrow], wqr[0]);
    fma8(acc, L.xn[qrow + 64], wqr[1]);
    fma8(acc, tid < 128 ? L.xn[qrow + 128] : 0.f, wqr[2]);
#pragma unroll
    for (int k = 0; k < 8; ++k) { float a = acc[k]; a += dpp<ROR8>(a); acc[k] = xrow16_sum(a); }
    if (lane < 8) {
      *(f32x4*)&L.qred[wave][lane * 8] = f32x4{acc[0], acc[1], acc[2], acc[3]};
      *(f32x4*)&L.qred[wave][lane * 8 + 4] = f32x4{acc[4], acc[5], acc[6], acc[7]};
    }
  }
  if (tid < 432) {
    float ag[8], au[8];
#pragma unroll
    for (int k = 0; k < 8; ++k) { ag[k] = 0.f; au[k] = 0.f; }
#pragma unroll
    for (int j = 0; j < 3; ++j) { const float cf = L.xn[grow + 24 * j]; fma8(ag, cf, wgr[j]); fma8(au, cf, wur[j]); }
    *(f32x4*)&L.gr[0][grg][gch * 8] = f32x4{ag[0], ag[1], ag[2], ag[3]};
    *(f32x4*)&L.gr[0][grg][gch * 8 + 4] = f32x4{ag[4], ag[5], ag[6], ag[7]};
    *(f32x4*)&L.gr[1][grg][gch * 8] = f32x4{au[0], au[1], au[2], au[3]};
    *(f32x4*)&L.gr[1][grg][gch * 8 + 4] = f32x4{au[4], au[5], au[6], au[7]};
  }
  __syncthreads();
  if (tid < 32) {
    float a0 = 0.f, a1 = 0.f;
#pragma unroll
    for (int w = 0; w < 8; ++w) { a0 += L.qred[w][2 * tid]; a1 += L.qred[w][2 * tid + 1]; }
    stgran(rs_ws, WS_Q + (g * 512 + (s >> 2) * 128 + (s & 3) * 32 + tid) * 16, a0, a1, t0, t1);
  } else if (tid >= 64 && tid < 208) {
    const int col = tid - 64;
    float a0 = 0.f, a1 = 0.f;
#pragma unroll
    for (int q = 0; q < 24; ++q) { a0 += L.gr[0][q][col]; a1 += L.gr[1][q][col]; }
    stgran(rs_ws, WS_G + ((((cb & ~7) + col / 18) * 8 + (cb & 7)) * 18 + col % 18) * 16, a0, a1, t0, t1);
  }
  __builtin_amdgcn_sched_barrier(0);
  STAMP(2);

#pragma unroll
  for (int i = KI; i < 8; ++i) kr[i] = ldnt128(rs_k, kvoff + i * 4 * DH * 2);
#pragma unroll
  for (int i = 0; i < 8; ++i) vr[i] = ldnt128(rs_v, kvoff + i * 4 * DH * 2);
  __builtin_amdgcn_sched_barrier(0);

  {
    const v4u gq = poll_gran(rs_ws, WS_Q + (g * 512 + tid) * 16, true, t0, t1);
    *(v2u*)&L.qp[tid >> 7][(tid & 127) * 2] = v2u{gq.x, gq.z};
  }
  __syncthreads();
  if (tid < 128) {
    const float a0 = (L.qp[0][2 * tid] + L.qp[1][2 * tid]) + (L.qp[2][2 * tid] + L.qp[3][2 * tid]);
    const float a1 = (L.qp[0][2 * tid + 1] + L.qp[1][2 * tid + 1]) + (L.qp[2][2 * tid + 1] + L.qp[3][2 * tid + 1]);
    L.qs[tid] = (unsigned)f2bf_bits(a0) | ((unsigned)f2bf_bits(a1) << 16);
  }
  __syncthreads();
  STAMP(3);

  {
    const v4u q0 = *(const v4u*)&L.qs[c * 4], q1 = *(const v4u*)&L.qs[64 + c * 4];
    float xs[16];
#pragma unroll
    for (int i = 0; i < 8; ++i) {
      float d0 = 0.f, d1 = 0.f;
#pragma unroll
      for (int k = 0; k < 4; ++k) { d0 = dot2(kr[i][k], q0[k], d0); d1 = dot2(kr[i][k], q1[k], d1); }
      xs[2 * i] = d0; xs[2 * i + 1] = d1;
    }
#define TR_STEP(HALF, CTRL, BIT) _Pragma("unroll") for (int h = 0; h < (HALF); ++h) { \
      const float lo_ = xs[h], hi_ = xs[h + (HALF)]; const float t1_ = lo_ + dpp<CTRL>(lo_), t2_ = hi_ + dpp<CTRL>(hi_); \
      xs[h] = (c & (BIT)) ? t2_ : t1_; }
    TR_STEP(8, ROR8, 8) TR_STEP(4, XOR7, 4) TR_STEP(2, XOR2, 2) TR_STEP(1, XOR1, 1)
#undef TR_STEP
    const float sc = rbf(rbf(xs[0]) * 0.08838834764831845f) * 1.4426950408889634f;
    float m = sc;
    m = fmaxf(m, dpp<XOR2>(m)); m = fmaxf(m, dpp<ROR4>(m)); m = fmaxf(m, dpp<ROR8>(m)); m = xrow16_max(m);
    const float p = __builtin_amdgcn_exp2f(sc - m);
    float l = p;
    l += dpp<XOR2>(l); l += dpp<ROR4>(l); l += dpp<ROR8>(l); l = xrow16_sum(l);
    L.pb[wave][lane] = p;
    __builtin_amdgcn_wave_barrier();
    asm volatile("s_waitcnt lgkmcnt(0)" ::: "memory");
    f32x4 pr[4];
#pragma unroll
    for (int u = 0; u < 4; ++u) pr[u] = *(const f32x4*)&L.pb[wave][r * 16 + u * 4];
    float a0[8], a1[8];
#pragma unroll
    for (int k = 0; k < 8; ++k) { a0[k] = 0.f; a1[k] = 0.f; }
#pragma unroll
    for (int u = 0; u < 4; ++u) {
      const unsigned pp0 = cvtpk(pr[u][0], pr[u][2]), pp1 = cvtpk(pr[u][1], pr[u][3]);
#pragma unroll
      for (int k = 0; k < 4; ++k) {
        const unsigned lo = __builtin_amdgcn_perm(vr[2 * u + 1][k], vr[2 * u][k], 0x05040100u);
        const unsigned hi = __builtin_amdgcn_perm(vr[2 * u + 1][k], vr[2 * u][k], 0x07060302u);
        a0[2 * k] = dot2(lo, pp0, a0[2 * k]); a0[2 * k + 1] = dot2(hi, pp0, a0[2 * k + 1]);
        a1[2 * k] = dot2(lo, pp1, a1[2 * k]); a1[2 * k + 1] = dot2(hi, pp1, a1[2 * k + 1]);
      }
    }
    float bs[8];
#pragma unroll
    for (int k = 0; k < 8; ++k) {
      auto t = __builtin_amdgcn_permlane32_swap(__float_as_uint(a0[k]), __float_as_uint(a1[k]), false, false);
      bs[k] = __uint_as_float(t[0]) + __uint_as_float(t[1]);
    }
    f32x4 o4;
#pragma unroll
    for (int k = 0; k < 4; ++k) {
      auto t = __builtin_amdgcn_permlane16_swap(__float_as_uint(bs[k]), __float_as_uint(bs[k + 4]), false, false);
      o4[k] = __uint_as_float(t[0]) + __uint_as_float(t[1]);
    }
    *(f32x4*)&L.mrg[wave][r >> 1][4 + c * 8 + (r & 1) * 4] = o4;
    if (r == 0) {
      if (c < 2) { L.mrg[wave][c][0] = m; L.mrg[wave][c][1] = l; }
    }
  }
  v4u wdr[3], wor[3];
  const int chunk = tid % 72, rg = tid / 72;
  {
    const bool act = tid < 432;
#pragma unroll
    for (int j = 0; j < 3; ++j) {
      const int row = rg + 6 * j;
      wor[j] = ldnt128(rs_wo, (act && row < 16) ? ((cb * 16 + row) * D + chunk * 8) * 2 : OOB);
      wdr[j] = ldnt128(rs_wd, act ? ((cb * 18 + row) * D + chunk * 8) * 2 : OOB);
    }
  }
  __builtin_amdgcn_sched_barrier(0);
  __syncthreads();
  STAMP(4);
  if (tid < 144) {
    const int h = tid < 128 ? (tid >> 6) : ((tid - 128) >> 3);
    float M = -INFINITY;
#pragma unroll
    for (int w = 0; w < 8; ++w) M = fmaxf(M, L.mrg[w][h][0]);
    float e[8];
#pragma unroll
    for (int w = 0; w < 8; ++w) e[w] = __builtin_amdgcn_exp2f(L.mrg[w][h][0] - M);
    if (tid < 128) {
      const int dp = tid & 63;
      float o0 = 0.f, o1 = 0.f;
#pragma unroll
      for (int w = 0; w < 8; ++w) { o0 = fmaf(e[w], L.mrg[w][h][4 + 2 * dp], o0); o1 = fmaf(e[w], L.mrg[w][h][5 + 2 * dp], o1); }
      const int cons = h * 8 + (dp >> 3);
      stgran(rs_ws, WS_A + (((g * 16 + cons) * 16 + s) * 9 + 1 + (dp & 7)) * 16, o0, o1, t0, t1);
    } else {
      float Ls = 0.f;
#pragma unroll
      for (int w = 0; w < 8; ++w) Ls = fmaf(e[w], L.mrg[w][h][1], Ls);
      stgran(rs_ws, WS_A + (((g * 16 + (tid - 128)) * 16 + s) * 9) * 16, M, Ls, t0, t1);
    }
  } else if (tid >= 192 && tid < 384) {
    const int t = tid - 192;
    const v4u gg = poll_gran(rs_ws, WS_G + (cb * 144 + t) * 16, t < 144, t0, t1);
    if (t < 144) { L.gh[t / 18][(t % 18) * 2] = __uint_as_float(gg.x); L.gh[t / 18][(t % 18) * 2 + 1] = __uint_as_float(gg.z); }
  }
  STAMP(5);
  __syncthreads();
  if (tid < 18) {
    float gt = 0.f, up = 0.f;
#pragma unroll
    for (int p = 0; p < 8; ++p) { gt += L.gh[p][2 * tid]; up += L.gh[p][2 * tid + 1]; }
    gt = rbf(gt); up = rbf(up);
    L.hs[tid] = rbf((gt > 0.f ? gt : 0.f) * up);
  }
  STAMP(6);
  if (wave >= 1 && wave < 4) {
    const int t = tid - 64;
    const v4u ga = poll_gran(rs_ws, WS_A + (cb * 144 + t) * 16, t < 144, t0, t1);
    if (t < 144) { L.ga[t / 9][(t % 9) * 2] = __uint_as_float(ga.x); L.ga[t / 9][(t % 9) * 2 + 1] = __uint_as_float(ga.z); }
  }
  __syncthreads();
  if (tid < 16) {
    float M = -INFINITY;
#pragma unroll
    for (int p = 0; p < 16; ++p) M = fmaxf(M, L.ga[p][0]);
    float Ls = 0.f, o = 0.f;
#pragma unroll
    for (int p = 0; p < 16; ++p) { const float e = __builtin_amdgcn_exp2f(L.ga[p][0] - M); Ls = fmaf(e, L.ga[p][1], Ls); o = fmaf(e, L.ga[p][2 + tid], o); }
    L.oc[tid] = rbf(o / Ls);
  }
  __syncthreads();
  STAMP(7);
  if (tid < 432) {
    float acc[8];
#pragma unroll
    for (int k = 0; k < 8; ++k) acc[k] = 0.f;
#pragma unroll
    for (int j = 0; j < 3; ++j) {
      const int row = rg + 6 * j;
      fma8(acc, L.hs[row], wdr[j]);
      fma8(acc, row < 16 ? L.oc[row] : 0.f, wor[j]);
    }
    *(f32x4*)&L.fr[rg][chunk * 8] = f32x4{acc[0], acc[1], acc[2], acc[3]};
    *(f32x4*)&L.fr[rg][chunk * 8 + 4] = f32x4{acc[4], acc[5], acc[6], acc[7]};
  }
  __syncthreads();
  if (tid < 288) {
    float a0 = 0.f, a1 = 0.f;
#pragma unroll
    for (int q = 0; q < 6; ++q) { a0 += L.fr[q][2 * tid]; a1 += L.fr[q][2 * tid + 1]; }
    stgran(rs_ws, WS_F + (((tid >> 1) * 256 + b) * 2 + (tid & 1)) * 16, a0, a1, t0, t1);
  }
  STAMP(8);
  if (b < NRED) {
    __builtin_amdgcn_s_sleep(FSLEEP);
    const v4u gf = poll_gran(rs_ws, WS_F + (b * 512 + tid) * 16, true, t0, t1);
    float a0 = __uint_as_float(gf.x), a1 = __uint_as_float(gf.z);
    a0 += dpp<XOR2>(a0); a0 += dpp<ROR4>(a0); a0 += dpp<ROR8>(a0); a0 = xrow16_sum(a0);
    a1 += dpp<XOR2>(a1); a1 += dpp<ROR4>(a1); a1 += dpp<ROR8>(a1); a1 = xrow16_sum(a1);
    if (lane < 2) { L.fin[wave][lane * 2] = a0; L.fin[wave][lane * 2 + 1] = a1; }
    __syncthreads();
    if (tid < 4) {
      float t = 0.f;
#pragma unroll
      for (int w = 0; w < 8; ++w) t += L.fin[w][tid];
      out[b * 4 + tid] = f2bf_bits(bf2f(x[b * 4 + tid]) + t);
    }
    if (b == 0 && tid == 0) *(volatile unsigned long long*)ws = nonce;
  }
  STAMP(9); STAMP(10); STAMP(11);
}
}

extern "C" void kernel_launch(void* const* d_in, const int* in_sizes, int n_in, void* d_out, int out_size, void* d_ws,
                              size_t ws_size, hipStream_t stream) {
  (void)in_sizes; (void)n_in; (void)out_size; (void)ws_size;
  fused_kernel<<<NWG, NT, 0, stream>>>((const unsigned short*)d_in[0], (const unsigned short*)d_in[1], (const unsigned short*)d_in[2],
                                       (const unsigned short*)d_in[3], (const unsigned short*)d_in[4], (const unsigned short*)d_in[6],
                                       (const unsigned short*)d_in[7], (const unsigned short*)d_in[8], (unsigned short*)d_out,
                                       (unsigned char*)d_ws);
}
```

```cpp
#include <hip/hip_runtime.h>
#include <hip/hip_bf16.h>
#include <stdint.h>

typedef unsigned v4u __attribute__((ext_vector_type(4)));
typedef unsigned v2u __attribute__((ext_vector_type(2)));
typedef float f32x4 __attribute__((ext_vector_type(4)));
typedef __attribute__((ext_vector_type(2))) __bf16 bf16x2;

namespace {
constexpr int D = 576, HKV = 16, SK = 32768, DH = 128, WIN = 4096, FF = 4608;
constexpr int NWG = 256, NT = 512;
constexpr int NRED = 144;
constexpr int WS_Q = 4096;
constexpr int WS_G = WS_Q + 16 * 512 * 16;
constexpr int WS_A = WS_G + 256 * 144 * 16;
constexpr int WS_F = WS_A + 256 * 144 * 16;
constexpr int WS_END = WS_F + NRED * 512 * 16;
constexpr int RSRC_W3 = 0x00020000;
constexpr int AUX_NT = 2, AUX_SC1 = 16;
constexpr unsigned SPIN_CAP = 1u << 20;
constexpr int OOB = 0x7ffffff0;

__device__ __forceinline__ float bf2f(unsigned short b) { return __builtin_bit_cast(float, (unsigned)b << 16); }
__device__ __forceinline__ float lo2f(unsigned w) { return __builtin_bit_cast(float, w << 16); }
__device__ __forceinline__ float hi2f(unsigned w) { return __builtin_bit_cast(float, w & 0xffff0000u); }
__device__ __forceinline__ unsigned short f2bf_bits(float f) { return __builtin_bit_cast(unsigned short, __float2bfloat16(f)); }
__device__ __forceinline__ float rbf(float f) { return bf2f(f2bf_bits(f)); }
__device__ __forceinline__ unsigned cvtpk(float lo, float hi) {
  return (unsigned)f2bf_bits(lo) | ((unsigned)f2bf_bits(hi) << 16);
}
__device__ __forceinline__ float dot2(unsigned k, unsigned q, float acc) {
  return __builtin_amdgcn_fdot2_f32_bf16(__builtin_bit_cast(bf16x2, k), __builtin_bit_cast(bf16x2, q), acc, false);
}
template <int CTRL> __device__ __forceinline__ float dpp(float x) {
  return __builtin_bit_cast(float, __builtin_amdgcn_mov_dpp(__builtin_bit_cast(int, x), CTRL, 0xf, 0xf, true));
}
constexpr int XOR1 = 0xB1, XOR2 = 0x4E, XOR7 = 0x141, ROR8 = 0x128, ROR4 = 0x124, ROR2 = 0x122, ROR1 = 0x121;
__device__ __forceinline__ float xrow16_max(float x) {
  auto s = __builtin_amdgcn_permlane16_swap(__float_as_uint(x), __float_as_uint(x), false, false);
  x = fmaxf(__uint_as_float(s[0]), __uint_as_float(s[1]));
  auto t = __builtin_amdgcn_permlane32_swap(__float_as_uint(x), __float_as_uint(x), false, false);
  return fmaxf(__uint_as_float(t[0]), __uint_as_float(t[1]));
}
__device__ __forceinline__ float xrow16_sum(float x) {
  auto s = __builtin_amdgcn_permlane16_swap(__float_as_uint(x), __float_as_uint(x), false, false);
  x = __uint_as_float(s[0]) + __uint_as_float(s[1]);
  auto t = __builtin_amdgcn_permlane32_swap(__float_as_uint(x), __float_as_uint(x), false, false);
  return __uint_as_float(t[0]) + __uint_as_float(t[1]);
}
__device__ __forceinline__ float row16_sum(float x) {
  x += dpp<ROR8>(x); x += dpp<ROR4>(x); x += dpp<ROR2>(x); x += dpp<ROR1>(x);
  return x;
}
__device__ __forceinline__ float wave_sum(float x) { return xrow16_sum(row16_sum(x)); }
__device__ __forceinline__ __amdgpu_buffer_rsrc_t mkrs(const void* p, unsigned bytes) {
  return __builtin_amdgcn_make_buffer_rsrc((void*)p, 0, (int)bytes, RSRC_W3);
}
__device__ __forceinline__ v4u ldnt128(__amdgpu_buffer_rsrc_t rs, int off) { return __builtin_amdgcn_raw_buffer_load_b128(rs, off, 0, AUX_NT); }
__device__ __forceinline__ v4u ldsc(__amdgpu_buffer_rsrc_t rs, int off) { return __builtin_amdgcn_raw_buffer_load_b128(rs, off, 0, AUX_SC1); }
__device__ __forceinline__ void stgran(__amdgpu_buffer_rsrc_t rs, int off, float a, float b, unsigned t0, unsigned t1) {
  v4u g = {__float_as_uint(a), t0, __float_as_uint(b), t1};
  __builtin_amdgcn_raw_buffer_store_b128(g, rs, off, 0, AUX_SC1);
}
__device__ __forceinline__ v4u poll_gran(__amdgpu_buffer_rsrc_t rs, int off, bool active, unsigned t0, unsigned t1) {
  v4u g = ldsc(rs, off);
  bool ok = !active || (g.y == t0 && g.w == t1);
  for (unsigned spins = 0; spins < SPIN_CAP; ++spins) {
    if (__all(ok)) break;
    __builtin_amdgcn_s_sleep(1);
    if (!ok) { g = ldsc(rs, off); ok = (g.y == t0 && g.w == t1); }
  }
  return g;
}
__device__ __forceinline__ void fma8(float (&acc)[8], float c, const v4u& w) {
#pragma unroll
  for (int k = 0; k < 4; ++k) { acc[2 * k] = fmaf(c, lo2f(w[k]), acc[2 * k]); acc[2 * k + 1] = fmaf(c, hi2f(w[k]), acc[2 * k + 1]); }
}

struct Lds {
  float xn[D];
  float qred[8][64];
  float gr[2][24][144];
  float qp[4][256];
  unsigned qs[128];
  float pb[8][64];
  float mrg[8][2][132];
  float gh[8][36];
  float hs[18];
  float ga[16][18];
  float oc[16];
  float fr[6][D];
  float fin[8][4];
};

__global__ void __launch_bounds__(NT, 2)
fused_kernel(const unsigned short* __restrict__ x, const unsigned short* __restrict__ kc, const unsigned short* __restrict__ vc,
             const unsigned short* __restrict__ wq, const unsigned short* __restrict__ wo, const unsigned short* __restrict__ wg,
             const unsigned short* __restrict__ wu, const unsigned short* __restrict__ wd, unsigned short* __restrict__ out,
             unsigned char* ws) {
  __shared__ __attribute__((aligned(16))) Lds L;
  const int tid = threadIdx.x, lane = tid & 63, wave = __builtin_amdgcn_readfirstlane(tid >> 6);
  const int b = blockIdx.x, cb = (b & 7) * 32 + (b >> 3), g = cb >> 4, s = cb & 15;
  const int r = lane >> 4, c = lane & 15;

  const __amdgpu_buffer_rsrc_t rs_ws = mkrs(ws, WS_END);
  const __amdgpu_buffer_rsrc_t rs_k = mkrs(kc, (unsigned)HKV * SK * DH * 2u);
  const __amdgpu_buffer_rsrc_t rs_v = mkrs(vc, (unsigned)HKV * SK * DH * 2u);
  const __amdgpu_buffer_rsrc_t rs_wq = mkrs(wq, D * 4096 * 2);
  const __amdgpu_buffer_rsrc_t rs_wo = mkrs(wo, 4096 * D * 2);
  const __amdgpu_buffer_rsrc_t rs_wg = mkrs(wg, D * FF * 2);
  const __amdgpu_buffer_rsrc_t rs_wu = mkrs(wu, D * FF * 2);
  const __amdgpu_buffer_rsrc_t rs_wd = mkrs(wd, FF * D * 2);

  unsigned short xr[9];
#pragma unroll
  for (int j = 0; j < 9; ++j) xr[j] = x[lane + 64 * j];
  __builtin_amdgcn_sched_barrier(0);
  const int qrow = (s >> 2) * 144 + (tid >> 3);
  v4u wqr[3];
  {
    const int o = (qrow * 4096 + g * 256 + (s & 3) * 64 + (tid & 7) * 8) * 2;
    wqr[0] = ldnt128(rs_wq, o);
    wqr[1] = ldnt128(rs_wq, o + 64 * 4096 * 2);
    wqr[2] = ldnt128(rs_wq, tid < 128 ? o + 128 * 4096 * 2 : OOB);
  }
  const v2u nraw = __builtin_amdgcn_raw_buffer_load_b64(rs_ws, 0, 0, 0);
  const int gch = tid % 18, grg = tid / 18;
  const int grow = (cb & 7) * 72 + grg;
  v4u wgr[3], wur[3];
  {
    const int o = (grow * FF + (cb >> 3) * 144 + gch * 8) * 2;
#pragma unroll
    for (int j = 0; j < 3; ++j) {
      wgr[j] = ldnt128(rs_wg, tid < 432 ? o + j * 24 * FF * 2 : OOB);
      wur[j] = ldnt128(rs_wu, tid < 432 ? o + j * 24 * FF * 2 : OOB);
    }
  }
  const int kvoff = (int)((((unsigned)g * SK + (SK - WIN) + s * 256 + wave * 32 + r) * DH + c * 8) * 2u);
  v4u kr[8], vr[8];
#pragma unroll
  for (int i = 0; i < 8; ++i) kr[i] = ldnt128(rs_k, kvoff + i * 4 * DH * 2);
  __builtin_amdgcn_sched_barrier(0);

  {
    float xv[9];
#pragma unroll
    for (int j = 0; j < 9; ++j) xv[j] = bf2f(xr[j]);
    float sm = 0.f;
#pragma unroll
    for (int j = 0; j < 9; ++j) sm += xv[j];
    const float mean = wave_sum(sm) * (1.0f / D);
    float sv = 0.f;
#pragma unroll
    for (int j = 0; j < 9; ++j) { const float d = xv[j] - mean; sv += d * d; }
    const float var = wave_sum(sv) * (1.0f / D);
    const float mb = rbf(mean), vb = rbf(var);
    const float den = rbf(sqrtf(rbf(vb + 1e-5f)));
#pragma unroll
    for (int j = 0; j < 9; ++j) L.xn[lane + 64 * j] = rbf(rbf(xv[j] - mb) / den);
    __builtin_amdgcn_wave_barrier();
    asm volatile("s_waitcnt lgkmcnt(0)" ::: "memory");
  }
  const unsigned long long nonce = (((unsigned long long)nraw.y << 32) | nraw.x) + 1ull;
  const unsigned t0 = (unsigned)nonce, t1 = (unsigned)(nonce >> 32);

  {
    float acc[8];
#pragma unroll
    for (int k = 0; k < 8; ++k) acc[k] = 0.f;
    fma8(acc, L.xn[qrow], wqr[0]);
    fma8(acc, L.xn[qrow + 64], wqr[1]);
    fma8(acc, tid < 128 ? L.xn[qrow + 128] : 0.f, wqr[2]);
#pragma unroll
    for (int k = 0; k < 8; ++k) { float a = acc[k]; a += dpp<ROR8>(a); acc[k] = xrow16_sum(a); }
    if (lane < 8) {
      *(f32x4*)&L.qred[wave][lane * 8] = f32x4{acc[0], acc[1], acc[2], acc[3]};
      *(f32x4*)&L.qred[wave][lane * 8 + 4] = f32x4{acc[4], acc[5], acc[6], acc[7]};
    }
  }
  if (tid < 432) {
    float ag[8], au[8];
#pragma unroll
    for (int k = 0; k < 8; ++k) { ag[k] = 0.f; au[k] = 0.f; }
#pragma unroll
    for (int j = 0; j < 3; ++j) { const float cf = L.xn[grow + 24 * j]; fma8(ag, cf, wgr[j]); fma8(au, cf, wur[j]); }
    *(f32x4*)&L.gr[0][grg][gch * 8] = f32x4{ag[0], ag[1], ag[2], ag[3]};
    *(f32x4*)&L.gr[0][grg][gch * 8 + 4] = f32x4{ag[4], ag[5], ag[6], ag[7]};
    *(f32x4*)&L.gr[1][grg][gch * 8] = f32x4{au[0], au[1], au[2], au[3]};
    *(f32x4*)&L.gr[1][grg][gch * 8 + 4] = f32x4{au[4], au[5], au[6], au[7]};
  }
  __syncthreads();
  if (tid < 32) {
    float a0 = 0.f, a1 = 0.f;
#pragma unroll
    for (int w = 0; w < 8; ++w) { a0 += L.qred[w][2 * tid]; a1 += L.qred[w][2 * tid + 1]; }
    stgran(rs_ws, WS_Q + (g * 512 + (s >> 2) * 128 + (s & 3) * 32 + tid) * 16, a0, a1, t0, t1);
  } else if (tid >= 64 && tid < 208) {
    const int col = tid - 64;
    float a0 = 0.f, a1 = 0.f;
#pragma unroll
    for (int q = 0; q < 24; ++q) { a0 += L.gr[0][q][col]; a1 += L.gr[1][q][col]; }
    stgran(rs_ws, WS_G + ((((cb & ~7) + col / 18) * 8 + (cb & 7)) * 18 + col % 18) * 16, a0, a1, t0, t1);
  }
  __builtin_amdgcn_sched_barrier(0);

#pragma unroll
  for (int i = 0; i < 8; ++i) vr[i] = ldnt128(rs_v, kvoff + i * 4 * DH * 2);
  __builtin_amdgcn_sched_barrier(0);

  {
    const v4u gq = poll_gran(rs_ws, WS_Q + (g * 512 + tid) * 16, true, t0, t1);
    *(v2u*)&L.qp[tid >> 7][(tid & 127) * 2] = v2u{gq.x, gq.z};
  }
  __syncthreads();
  if (tid < 128) {
    const float a0 = (L.qp[0][2 * tid] + L.qp[1][2 * tid]) + (L.qp[2][2 * tid] + L.qp[3][2 * tid]);
    const float a1 = (L.qp[0][2 * tid + 1] + L.qp[1][2 * tid + 1]) + (L.qp[2][2 * tid + 1] + L.qp[3][2 * tid + 1]);
    L.qs[tid] = (unsigned)f2bf_bits(a0) | ((unsigned)f2bf_bits(a1) << 16);
  }
  __syncthreads();

  {
    const v4u q0 = *(const v4u*)&L.qs[c * 4], q1 = *(const v4u*)&L.qs[64 + c * 4];
    float xs[16];
#pragma unroll
    for (int i = 0; i < 8; ++i) {
      float d0 = 0.f, d1 = 0.f;
#pragma unroll
      for (int k = 0; k < 4; ++k) { d0 = dot2(kr[i][k], q0[k], d0); d1 = dot2(kr[i][k], q1[k], d1); }
      xs[2 * i] = d0; xs[2 * i + 1] = d1;
    }
#define TR_STEP(HALF, CTRL, BIT) _Pragma("unroll") for (int h = 0; h < (HALF); ++h) { \
      const float lo_ = xs[h], hi_ = xs[h + (HALF)]; const float t1_ = lo_ + dpp<CTRL>(lo_), t2_ = hi_ + dpp<CTRL>(hi_); \
      xs[h] = (c & (BIT)) ? t2_ : t1_; }
    TR_STEP(8, ROR8, 8) TR_STEP(4, XOR7, 4) TR_STEP(2, XOR2, 2) TR_STEP(1, XOR1, 1)
#undef TR_STEP
    const float sc = rbf(rbf(xs[0]) * 0.08838834764831845f) * 1.4426950408889634f;
    float m = sc;
    m = fmaxf(m, dpp<XOR2>(m)); m = fmaxf(m, dpp<ROR4>(m)); m = fmaxf(m, dpp<ROR8>(m)); m = xrow16_max(m);
    const float p = __builtin_amdgcn_exp2f(sc - m);
    float l = p;
    l += dpp<XOR2>(l); l += dpp<ROR4>(l); l += dpp<ROR8>(l); l = xrow16_sum(l);
    L.pb[wave][lane] = p;
    __builtin_amdgcn_wave_barrier();
    asm volatile("s_waitcnt lgkmcnt(0)" ::: "memory");
    f32x4 pr[4];
#pragma unroll
    for (int u = 0; u < 4; ++u) pr[u] = *(const f32x4*)&L.pb[wave][r * 16 + u * 4];
    float a0[8], a1[8];
#pragma unroll
    for (int k = 0; k < 8; ++k) { a0[k] = 0.f; a1[k] = 0.f; }
#pragma unroll
    for (int u = 0; u < 4; ++u) {
      const unsigned pp0 = cvtpk(pr[u][0], pr[u][2]), pp1 = cvtpk(pr[u][1], pr[u][3]);
#pragma unroll
      for (int k = 0; k < 4; ++k) {
        const unsigned lo = __builtin_amdgcn_perm(vr[2 * u + 1][k], vr[2 * u][k], 0x05040100u);
        const unsigned hi = __builtin_amdgcn_perm(vr[2 * u + 1][k], vr[2 * u][k], 0x07060302u);
        a0[2 * k] = dot2(lo, pp0, a0[2 * k]); a0[2 * k + 1] = dot2(hi, pp0, a0[2 * k + 1]);
        a1[2 * k] = dot2(lo, pp1, a1[2 * k]); a1[2 * k + 1] = dot2(hi, pp1, a1[2 * k + 1]);
      }
    }
    float bs[8];
#pragma unroll
    for (int k = 0; k < 8; ++k) {
      auto t = __builtin_amdgcn_permlane32_swap(__float_as_uint(a0[k]), __float_as_uint(a1[k]), false, false);
      bs[k] = __uint_as_float(t[0]) + __uint_as_float(t[1]);
    }
    f32x4 o4;
#pragma unroll
    for (int k = 0; k < 4; ++k) {
      auto t = __builtin_amdgcn_permlane16_swap(__float_as_uint(bs[k]), __float_as_uint(bs[k + 4]), false, false);
      o4[k] = __uint_as_float(t[0]) + __uint_as_float(t[1]);
    }
    *(f32x4*)&L.mrg[wave][r >> 1][4 + c * 8 + (r & 1) * 4] = o4;
    if (r == 0) {
      if (c < 2) { L.mrg[wave][c][0] = m; L.mrg[wave][c][1] = l; }
    }
  }
  v4u wdr[3], wor[3];
  const int chunk = tid % 72, rg = tid / 72;
  {
    const bool act = tid < 432;
#pragma unroll
    for (int j = 0; j < 3; ++j) {
      const int row = rg + 6 * j;
      wor[j] = ldnt128(rs_wo, (act && row < 16) ? ((cb * 16 + row) * D + chunk * 8) * 2 : OOB);
      wdr[j] = ldnt128(rs_wd, act ? ((cb * 18 + row) * D + chunk * 8) * 2 : OOB);
    }
  }
  __builtin_amdgcn_sched_barrier(0);
  __syncthreads();
  if (tid < 144) {
    const int h = tid < 128 ? (tid >> 6) : ((tid - 128) >> 3);
    float M = -INFINITY;
#pragma unroll
    for (int w = 0; w < 8; ++w) M = fmaxf(M, L.mrg[w][h][0]);
    float e[8];
#pragma unroll
    for (int w = 0; w < 8; ++w) e[w] = __builtin_amdgcn_exp2f(L.mrg[w][h][0] - M);
    if (tid < 128) {
      const int dp = tid & 63;
      float o0 = 0.f, o1 = 0.f;
#pragma unroll
      for (int w = 0; w < 8; ++w) { o0 = fmaf(e[w], L.mrg[w][h][4 + 2 * dp], o0); o1 = fmaf(e[w], L.mrg[w][h][5 + 2 * dp], o1); }
      const int cons = h * 8 + (dp >> 3);
      stgran(rs_ws, WS_A + (((g * 16 + cons) * 16 + s) * 9 + 1 + (dp & 7)) * 16, o0, o1, t0, t1);
    } else {
      float Ls = 0.f;
#pragma unroll
      for (int w = 0; w < 8; ++w) Ls = fmaf(e[w], L.mrg[w][h][1], Ls);
      stgran(rs_ws, WS_A + (((g * 16 + (tid - 128)) * 16 + s) * 9) * 16, M, Ls, t0, t1);
    }
  } else if (tid >= 192 && tid < 384) {
    const int t = tid - 192;
    const v4u gg = poll_gran(rs_ws, WS_G + (cb * 144 + t) * 16, t < 144, t0, t1);
    if (t < 144) { L.gh[t / 18][(t % 18) * 2] = __uint_as_float(gg.x); L.gh[t / 18][(t % 18) * 2 + 1] = __uint_as_float(gg.z); }
  }
  __syncthreads();
  if (tid < 18) {
    float gt = 0.f, up = 0.f;
#pragma unroll
    for (int p = 0; p < 8; ++p) { gt += L.gh[p][2 * tid]; up += L.gh[p][2 * tid + 1]; }
    gt = rbf(gt); up = rbf(up);
    L.hs[tid] = rbf((gt > 0.f ? gt : 0.f) * up);
  }
  if (wave >= 1 && wave < 4) {
    const int t = tid - 64;
    const v4u ga = poll_gran(rs_ws, WS_A + (cb * 144 + t) * 16, t < 144, t0, t1);
    if (t < 144) { L.ga[t / 9][(t % 9) * 2] = __uint_as_float(ga.x); L.ga[t / 9][(t % 9) * 2 + 1] = __uint_as_float(ga.z); }
  }
  __syncthreads();
  if (tid < 16) {
    float M = -INFINITY;
#pragma unroll
    for (int p = 0; p < 16; ++p) M = fmaxf(M, L.ga[p][0]);
    float Ls = 0.f, o = 0.f;
#pragma unroll
    for (int p = 0; p < 16; ++p) { const float e = __builtin_amdgcn_exp2f(L.ga[p][0] - M); Ls = fmaf(e, L.ga[p][1], Ls); o = fmaf(e, L.ga[p][2 + tid], o); }
    L.oc[tid] = rbf(o / Ls);
  }
  __syncthreads();
  if (tid < 432) {
    float acc[8];
#pragma unroll
    for (int k = 0; k < 8; ++k) acc[k] = 0.f;
#pragma unroll
    for (int j = 0; j < 3; ++j) {
      const int row = rg + 6 * j;
      fma8(acc, L.hs[row], wdr[j]);
      fma8(acc, row < 16 ? L.oc[row] : 0.f, wor[j]);
    }
    *(f32x4*)&L.fr[rg][chunk * 8] = f32x4{acc[0], acc[1], acc[2], acc[3]};
    *(f32x4*)&L.fr[rg][chunk * 8 + 4] = f32x4{acc[4], acc[5], acc[6], acc[7]};
  }
  __syncthreads();
  if (tid < 288) {
    float a0 = 0.f, a1 = 0.f;
#pragma unroll
    for (int q = 0; q < 6; ++q) { a0 += L.fr[q][2 * tid]; a1 += L.fr[q][2 * tid + 1]; }
    stgran(rs_ws, WS_F + (((tid >> 1) * 256 + b) * 2 + (tid & 1)) * 16, a0, a1, t0, t1);
  }
  if (b < NRED) {
    __builtin_amdgcn_s_sleep(32);
    const v4u gf = poll_gran(rs_ws, WS_F + (b * 512 + tid) * 16, true, t0, t1);
    float a0 = __uint_as_float(gf.x), a1 = __uint_as_float(gf.z);
    a0 += dpp<XOR2>(a0); a0 += dpp<ROR4>(a0); a0 += dpp<ROR8>(a0); a0 = xrow16_sum(a0);
    a1 += dpp<XOR2>(a1); a1 += dpp<ROR4>(a1); a1 += dpp<ROR8>(a1); a1 = xrow16_sum(a1);
    if (lane < 2) { L.fin[wave][lane * 2] = a0; L.fin[wave][lane * 2 + 1] = a1; }
    __syncthreads();
    if (tid < 4) {
      float t = 0.f;
#pragma unroll
      for (int w = 0; w < 8; ++w) t += L.fin[w][tid];
      out[b * 4 + tid] = f2bf_bits(bf2f(x[b * 4 + tid]) + t);
    }
    if (b == 0 && tid == 0) *(volatile unsigned long long*)ws = nonce;
  }
}
}

extern "C" void kernel_launch(void* const* d_in, const int* in_sizes, int n_in, void* d_out, int out_size, void* d_ws,
                              size_t ws_size, hipStream_t stream) {
  (void)in_sizes; (void)n_in; (void)out_size; (void)ws_size;
  fused_kernel<<<NWG, NT, 0, stream>>>((const unsigned short*)d_in[0], (const unsigned short*)d_in[1], (const unsigned short*)d_in[2],
                                       (const unsigned short*)d_in[3], (const unsigned short*)d_in[4], (const unsigned short*)d_in[6],
                                       (const unsigned short*)d_in[7], (const unsigned short*)d_in[8], (unsigned short*)d_out,
                                       (unsigned char*)d_ws);
}
```
